```python
import torch

def _softmax(x, axis=-1):
    return torch.softmax(x, dim=axis)
def _gelu(x):
    return torch.nn.functional.gelu(x, approximate="tanh")
def _silu(x):
    return torch.nn.functional.silu(x)
def _sigmoid(x):
    return torch.sigmoid(x)
def _layernorm(x, axis=-1, eps=1e-5):
    m = x.mean(dim=axis, keepdim=True)
    v = x.var(dim=axis, keepdim=True, unbiased=False)
    return (x - m) / torch.sqrt(v + eps)
def _scatter_add(tgt, idx, src, axis=0):
    return tgt.clone().index_add_(axis, idx, src)
def _segment_sum(src, seg, n):
    out = torch.zeros((n,) + src.shape[1:], dtype=src.dtype)
    return out.index_add_(0, seg, src)
def _pad(x, pw):
    flat = tuple(v for pair in reversed(pw) for v in pair)
    return torch.nn.functional.pad(x, flat)

kernel_name = 'parallel_sum__softmax_full_mha__glu_gated_swiglu__prompt'

def setup_inputs():

    _w = (lambda a: (lambda t: t.to(torch.bfloat16) if t.is_floating_point() else t)(torch.as_tensor(a)))
    def _A_setup():
        B, S, D = 1, 2048, 512
        return {
            "x": _w((lambda *s: torch.randn(*s, dtype=torch.bfloat16) * 0.05)(B, S, D)),
            "wqkv": _w((lambda *s: torch.randn(*s, dtype=torch.bfloat16) * 0.05)(D, 3 * D)),
            "wo": _w((lambda *s: torch.randn(*s, dtype=torch.bfloat16) * 0.05)(D, D)),
        }
    _A_in = _A_setup()

    def _B_setup():
        B, S, D, F = 1, 2048, 512, 1792
        return {
            "x": _w((lambda *s: torch.randn(*s, dtype=torch.bfloat16) * 0.05)(B, S, D)),
            "w1": _w((lambda *s: torch.randn(*s, dtype=torch.bfloat16) * 0.4)(D, F) * (2.0 / D) ** 0.5),
            "w3": _w((lambda *s: torch.randn(*s, dtype=torch.bfloat16) * 0.4)(D, F) * (2.0 / D) ** 0.5),
            "w2": _w((lambda *s: torch.randn(*s, dtype=torch.bfloat16) * 0.4)(F, D) * (2.0 / F) ** 0.5),
        }
    _B_in = _B_setup()

    return {
        "A_x": _A_in["x"],
        "A_wqkv": _A_in["wqkv"],
        "A_wo": _A_in["wo"],
        "B_x": _B_in["x"],
        "B_w1": _B_in["w1"],
        "B_w3": _B_in["w3"],
        "B_w2": _B_in["w2"],
    }

def reference(A_x, A_wqkv, A_wo, B_x, B_w1, B_w3, B_w2):
    def _A_ref(x, wqkv, wo):
        B, S, D = x.shape
        H = 32; Dh = D // H
        qkv = (lambda x, *d: x.permute(*d))((x @ wqkv).reshape(B, S, 3, H, Dh), 2, 0, 3, 1, 4)
        q, k, v = qkv[0], qkv[1], qkv[2]
        scale = Dh ** -0.5
        scores = torch.einsum("bhqd,bhkd->bhqk", q, k) * scale
        p = _softmax(scores, axis=-1)
        o = torch.einsum("bhqk,bhkd->bhqd", p, v)
        return (lambda x, *d: x.permute(*d))(o, 0, 2, 1, 3).reshape(B, S, D) @ wo

    def _B_ref(x, w1, w3, w2):
        gate = torch.einsum("bsd,df->bsf", x, w1)
        up = torch.einsum("bsd,df->bsf", x, w3)
        h = _silu(gate) * up
        out = torch.einsum("bsf,fd->bsd", h, w2)
        return out

    x = A_x
    xn = _layernorm(x, axis=-1)
    return x + _A_ref(xn, A_wqkv, A_wo) + _B_ref(xn, B_w1, B_w3, B_w2)
```

```cpp
#include <hip/hip_runtime.h>
#include <hip/hip_bf16.h>
#include <stdint.h>
#pragma clang diagnostic ignored "-Wint-to-pointer-cast"

typedef __bf16 bf16x2 __attribute__((ext_vector_type(2)));
typedef __bf16 bf16x8 __attribute__((ext_vector_type(8)));
typedef __bf16 bf16x16 __attribute__((ext_vector_type(16)));
typedef short s16x4 __attribute__((ext_vector_type(4)));
typedef float f32x2 __attribute__((ext_vector_type(2)));
typedef float f32x4 __attribute__((ext_vector_type(4)));
typedef float f32x8 __attribute__((ext_vector_type(8)));
typedef float f32x16 __attribute__((ext_vector_type(16)));
typedef unsigned u32x2 __attribute__((ext_vector_type(2)));
typedef unsigned u32x4 __attribute__((ext_vector_type(4)));
#define LAS __attribute__((address_space(3)))
#define DI __device__ __forceinline__

constexpr int SEQ = 2048, DM = 512, NH = 32, FF = 1792, NQKV = 1536;
constexpr int NFT = 144;
constexpr float LOG2E = 1.4426950408889634f;
constexpr float QSCALE = 0.25f * LOG2E;

constexpr size_t WS_Q = 0;
constexpr size_t WS_KV = WS_Q + (size_t)NH * SEQ * 16 * 2;
constexpr size_t WS_A2 = WS_KV + (size_t)NH * SEQ * 32 * 2;
constexpr size_t WS_KM = WS_A2 + (size_t)128 * NFT * 512;

#define STAMP(kid, i)
DI unsigned pk_bf16(float lo, float hi) { bf16x2 v = {(__bf16)lo, (__bf16)hi}; return __builtin_bit_cast(unsigned, v); }
DI float bf_lo(unsigned u) { return __uint_as_float(u << 16); }
DI float bf_hi(unsigned u) { return __uint_as_float(u & 0xffff0000u); }
DI float dot2(unsigned a, unsigned b, float c) { return __builtin_amdgcn_fdot2_f32_bf16(__builtin_bit_cast(bf16x2, a), __builtin_bit_cast(bf16x2, b), c, false); }
DI int lane_id() { int l; asm volatile("v_mbcnt_lo_u32_b32 %0, -1, 0\n\tv_mbcnt_hi_u32_b32 %0, -1, %0" : "=v"(l)); return l & 63; }
DI int rfl(int x) { return __builtin_amdgcn_readfirstlane(x); }
DI void dma16(unsigned voff, const void* sbase, unsigned ldsaddr) {
    asm volatile("s_mov_b32 m0, %2\n\ts_nop 0\n\tglobal_load_lds_dwordx4 %0, %1" ::"v"(voff), "s"(sbase), "s"(ldsaddr) : "memory");
}
#define WAIT_VM(n) asm volatile("s_waitcnt vmcnt(" #n ")" ::: "memory")
#define BARRIER() do { __builtin_amdgcn_s_waitcnt(0xC07F); asm volatile("s_waitcnt lgkmcnt(0)\n\ts_barrier" ::: "memory"); } while (0)
typedef LAS char* lptr;
DI u32x4 lds_ld16(unsigned a, int off) { return *(const LAS u32x4*)((lptr)a + off); }
DI u32x4 lds_tr16(unsigned a, int off, int off2) {
    const u32x2 lo = __builtin_bit_cast(u32x2, __builtin_amdgcn_ds_read_tr16_b64_v4i16((LAS s16x4*)((lptr)a + off)));
    const u32x2 hi = __builtin_bit_cast(u32x2, __builtin_amdgcn_ds_read_tr16_b64_v4i16((LAS s16x4*)((lptr)a + off2)));
    return u32x4{lo[0], lo[1], hi[0], hi[1]};
}
DI f32x4 mfma16(u32x4 a, u32x4 b, f32x4 c) {
    return __builtin_amdgcn_mfma_f32_16x16x32_bf16(__builtin_bit_cast(bf16x8, a), __builtin_bit_cast(bf16x8, b), c, 0, 0, 0);
}
DI float xsum16(float v) { return v + __shfl_xor(v, 16); }
DI float xsum32(float v) { return v + __shfl_xor(v, 32); }

constexpr int K1_WSLOT = 20480, K1_XSLOT = 16384;
constexpr int K1_W = 0, K1_X = 5 * K1_WSLOT, K1_STAT = K1_X + 3 * K1_XSLOT, K1_CS = K1_STAT + 1024, K1_LDS = K1_CS + 1280;

struct SlotSrc { int col; int kind; };
DI SlotSrc k1_slot(int cg, int s) {
    const int side = s >= 5, i2 = s - 5 * side;
    SlotSrc r;
    if (cg < 3) { r.col = cg * 320 + s * 32; r.kind = 0; }
    else if (cg == 3) { r.col = 1024 + s * 32; r.kind = 1; }
    else if (cg == 4) {
        if (i2 < 3) { r.col = 1344 + (side * 3 + i2) * 32; r.kind = 1; }
        else if (i2 == 3) { r.col = 960 + side * 32; r.kind = 0; }
        else { r.col = 1760; r.kind = 2; }
    } else { r.col = (cg - 5) * 160 + i2 * 32; r.kind = 2; }
    return r;
}

template <int MASK>
DI void k1_compute(int cg, int rb, int mh, int nh, char* ws) {
    int lane = lane_id(), fr = lane & 15, g = lane >> 4;
    const int q = (lane >> 2) & 3, p = lane & 3;
    const unsigned xb = K1_X + mh * 8192 + fr * 128 + ((g ^ (fr >> 1)) * 16);
    unsigned xA = xb + nh * 4096, xB = xb + (1 - nh) * 4096;
    const unsigned bj0 = (g >> 1) * 1024 + (8 * (g & 1) + q) * 64 + (((p >> 1) ^ ((g & 1) << 1)) * 16) + (p & 1) * 8, bj1 = bj0 ^ 32;
    const unsigned wE = nh ? bj1 + 4096 : bj0, wO = nh ? bj0 + 6144 : bj1;
    unsigned wa[4] = {wE + mh * 10240, wO + mh * 10240, wE + (1 - mh) * 10240, wO + (1 - mh) * 10240};
    f32x4 acc[4][10];
#pragma unroll
    for (int i = 0; i < 4; ++i)
#pragma unroll
        for (int t = 0; t < 10; ++t) acc[i][t] = f32x4{0.f, 0.f, 0.f, 0.f};
    float ssum[2] = {0.f, 0.f}, ssq[2] = {0.f, 0.f}, cs[5] = {0.f, 0.f, 0.f, 0.f, 0.f};
    u32x4 xf[4], xn[4], wf[10];
    auto rdx = [&](u32x4(&d)[4]) { d[0] = lds_ld16(xA, 0); d[1] = lds_ld16(xA, 2048); d[2] = lds_ld16(xB, 0); d[3] = lds_ld16(xB, 2048); };
    auto rdw = [&](int t) {
        const int tt = t % 5;
        wf[t] = lds_tr16(wa[(t / 5) * 2 + (tt & 1)], (tt >> 1) * 2048, (tt >> 1) * 2048 + 256);
    };
    STAMP(0, 0);
    BARRIER();
    STAMP(0, 1);
    rdx(xf);
#pragma unroll
    for (int t = 0; t < 10; ++t) rdw(t);
    int wslot = 0, xslot = 0;
    auto step = [&](u32x4(&xc)[4], u32x4(&xd)[4], int dx) {
        const int dw = wslot == 4 ? -4 * K1_WSLOT : K1_WSLOT;
        wslot = wslot == 4 ? 0 : wslot + 1;
        xA = (xA ^ 64) + dx; xB = (xB ^ 64) + dx;
#pragma unroll
        for (int i = 0; i < 4; ++i) wa[i] += dw;
        asm volatile("" : "+v"(xA), "+v"(xB), "+v"(wa[0]), "+v"(wa[1]), "+v"(wa[2]), "+v"(wa[3]));
        __builtin_amdgcn_sched_barrier(0);
        BARRIER();
        rdx(xd);
        __builtin_amdgcn_sched_barrier(0);
#pragma unroll
        for (int t = 0; t < 10; ++t) {
#pragma unroll
            for (int i = 0; i < 4; ++i) {
                if ((MASK >> t) & 1) acc[i][t] = mfma16(xc[i], wf[t], acc[i][t]);
                else acc[i][t] = mfma16(wf[t], xc[i], acc[i][t]);
            }
            if (t < 5) {
#pragma unroll
                for (int d = 0; d < 4; ++d) cs[t] = dot2(wf[t][d], 0x3f803f80u, cs[t]);
            } else if (t < 9) {
                const int i = (t - 5) >> 1, d0 = ((t - 5) & 1) * 2;
#pragma unroll
                for (int d = d0; d < d0 + 2; ++d) { ssum[i] = dot2(xc[i][d], 0x3f803f80u, ssum[i]); ssq[i] = dot2(xc[i][d], xc[i][d], ssq[i]); }
            }
            __builtin_amdgcn_sched_barrier(0);
            rdw(t);
            __builtin_amdgcn_sched_barrier(0);
        }
    };
    for (int u = 0; u < 8; ++u) {
        step(xf, xn, 0);
        step(xn, xf, xslot == 2 ? -2 * K1_XSLOT : K1_XSLOT);
        xslot = xslot == 2 ? 0 : xslot + 1;
    }
    STAMP(0, 2);
    lane = lane_id(); fr = lane & 15; g = lane >> 4;
#pragma unroll
    for (int i = 0; i < 2; ++i) {
        const float s = xsum32(xsum16(ssum[i])), s2 = xsum32(xsum16(ssq[i]));
        const float mean = s * (1.f / DM), var = fmaxf(s2 * (1.f / DM) - mean * mean, 0.f);
        if (g == 0) *(LAS f32x2*)(K1_STAT + (mh * 64 + (i + 2 * nh) * 16 + fr) * 8) = f32x2{mean, __builtin_amdgcn_rsqf(var + 1e-5f)};
    }
#pragma unroll
    for (int t = 0; t < 5; ++t) {
        const float s = xsum32(xsum16(cs[t]));
        if (g == 0) *(LAS float*)(K1_CS + ((mh * 10 + nh * 5 + t) * 16 + fr) * 4) = s;
    }
    BARRIER();
    STAMP(0, 3);
    __bf16* Qf = (__bf16*)(ws + WS_Q); __bf16* KV = (__bf16*)(ws + WS_KV);
    __bf16* A2 = (__bf16*)(ws + WS_A2); float* km = (float*)(ws + WS_KM);
    f32x2 st[4];
#pragma unroll
    for (int i = 0; i < 4; ++i) st[i] = *(const LAS f32x2*)(K1_STAT + (mh * 64 + (i ^ (2 * nh)) * 16 + fr) * 8);
    const int tok0 = rb * 128 + mh * 64;
#pragma unroll
    for (int tt = 0; tt < 5; ++tt) {
        const int it = nh * 5 + tt;
        const SlotSrc sa = k1_slot(cg, mh * 5 + (it >> 1)), sb = k1_slot(cg, (1 - mh) * 5 + (it >> 1));
        if ((MASK >> tt) & 1) {
#pragma unroll
            for (int sd = 0; sd < 2; ++sd) {
                const int t = tt + 5 * sd, col = (sd ? sb.col : sa.col) + (it & 1) * 16, h = (col - 1024) >> 4;
                const float c = *(const LAS float*)(K1_CS + ((((sd ^ mh) * 10) + it) * 16 + fr) * 4);
#pragma unroll
                for (int i = 0; i < 4; ++i) {
                    const int tl = mh * 64 + (i ^ (2 * nh)) * 16 + 4 * g;
                    const f32x4 sA = *(const LAS f32x4*)(K1_STAT + tl * 8), sB = *(const LAS f32x4*)(K1_STAT + tl * 8 + 16);
                    const float v0 = sA[1] * (acc[i][t][0] - sA[0] * c), v1 = sA[3] * (acc[i][t][1] - sA[2] * c);
                    const float v2 = sB[1] * (acc[i][t][2] - sB[0] * c), v3 = sB[3] * (acc[i][t][3] - sB[2] * c);
                    const int tok = rb * 128 + tl;
                    *(u32x2*)(KV + (size_t)h * 65536 + (tok >> 5) * 1024 + 512 + ((tok >> 3) & 1) * 256 + ((tok >> 4) & 1) * 128 + fr * 8 + (tok & 7)) = u32x2{pk_bf16(v0, v1), pk_bf16(v2, v3)};
                }
            }
        } else if (sa.kind == 2) {
            const f32x4 ca = *(const LAS f32x4*)(K1_CS + ((mh * 10 + it) * 16 + 4 * g) * 4), cb = *(const LAS f32x4*)(K1_CS + (((1 - mh) * 10 + it) * 16 + 4 * g) * 4);
            const int ft = 32 + (sa.col >> 4) + (it & 1);
#pragma unroll
            for (int i = 0; i < 4; ++i) {
                float hv[4];
#pragma unroll
                for (int r = 0; r < 4; ++r) {
                    const float a = st[i][1] * (acc[i][tt][r] - st[i][0] * ca[r]), b = st[i][1] * (acc[i][tt + 5][r] - st[i][0] * cb[r]);
                    const float gt = mh ? b : a;
                    hv[r] = a * b * __builtin_amdgcn_rcpf(1.f + __builtin_amdgcn_exp2f(-LOG2E * gt));
                }
                const int tb = (tok0 >> 4) + (i ^ (2 * nh));
                *(u32x2*)(A2 + ((size_t)tb * NFT + ft) * 256 + fr * 16 + 4 * g) = u32x2{pk_bf16(hv[0], hv[1]), pk_bf16(hv[2], hv[3])};
            }
        } else {
#pragma unroll
            for (int sd = 0; sd < 2; ++sd) {
                const int t = tt + 5 * sd, col = (sd ? sb.col : sa.col) + (it & 1) * 16;
                const f32x4 c = *(const LAS f32x4*)(K1_CS + ((((sd ^ mh) * 10) + it) * 16 + 4 * g) * 4);
                const bool isq = col < 512;
                const int h = (col >> 4) & 31;
                float kmx = 0.f;
#pragma unroll
                for (int i = 0; i < 4; ++i) {
                    const float sc = isq ? st[i][1] * QSCALE : st[i][1];
                    const u32x2 o = {pk_bf16(sc * (acc[i][t][0] - st[i][0] * c[0]), sc * (acc[i][t][1] - st[i][0] * c[1])),
                                     pk_bf16(sc * (acc[i][t][2] - st[i][0] * c[2]), sc * (acc[i][t][3] - st[i][0] * c[3]))};
                    const int tok = tok0 + (i ^ (2 * nh)) * 16 + fr;
                    if (isq) *(u32x2*)(Qf + (size_t)h * 32768 + tok * 16 + 4 * g) = o;
                    else {
                        *(u32x2*)(KV + (size_t)h * 65536 + (tok >> 5) * 1024 + (g >> 1) * 256 + (tok & 31) * 8 + (g & 1) * 4) = o;
                        const float a0 = bf_lo(o[0]), a1 = bf_hi(o[0]), a2 = bf_lo(o[1]), a3 = bf_hi(o[1]);
                        kmx = fmaxf(kmx, xsum32(xsum16(a0 * a0 + a1 * a1 + a2 * a2 + a3 * a3)));
                    }
                }
                if (!isq) {
                    kmx = fmaxf(kmx, __shfl_xor(kmx, 1)); kmx = fmaxf(kmx, __shfl_xor(kmx, 2));
                    kmx = fmaxf(kmx, __shfl_xor(kmx, 4)); kmx = fmaxf(kmx, __shfl_xor(kmx, 8));
                    if (lane == 0) km[h * 32 + (tok0 >> 6)] = kmx;
                }
            }
        }
    }
    STAMP(0, 4);
    STAMP(0, 5);
}

__global__ __launch_bounds__(512, 2) void k1_proj(const __bf16* __restrict__ x, const __bf16* __restrict__ wqkv, const __bf16* __restrict__ w1,
                                                  const __bf16* __restrict__ w3, char* __restrict__ ws) {
    extern __shared__ __attribute__((aligned(16))) char lds[];
    const int lane = threadIdx.x & 63, w = rfl(threadIdx.x >> 6);
    const int xcd = blockIdx.x & 7, j = blockIdx.x >> 3;
    const int rb = (xcd & 1) * 8 + (j & 7), cg = (xcd >> 1) * 4 + (j >> 3);
    if (w >= 4) {
        const int lw = w - 4;
        const char* sb[5]; unsigned vo[5]; int strd[5];
#pragma unroll
        for (int pp = 0; pp < 5; ++pp) {
            const int pi = 5 * lw + pp;
            const SlotSrc s = k1_slot(cg, pi >> 1);
            const int ld = s.kind == 2 ? FF : NQKV;
            const __bf16* src = s.kind == 2 ? (pi >= 10 ? w3 : w1) : wqkv;
            sb[pp] = (const char*)(src + s.col + (pi & 1) * 16 * ld); strd[pp] = 64 * ld;
            vo[pp] = ((lane >> 2) * ld + (((lane & 3) ^ ((lane >> 5) << 1)) * 8)) * 2;
        }
        const unsigned vx0 = (lane >> 3) * 1024 + (((lane & 7) ^ (lane >> 4)) * 16), vx1 = vx0 ^ 64;
        const char* xb = (const char*)(x + (size_t)(rb * 128 + lw * 32) * DM);
        unsigned wl = K1_W + lw * 5120, xl = K1_X + lw * 4096;
        auto ldw = [&]() {
#pragma unroll
            for (int pp = 0; pp < 5; ++pp) { dma16(vo[pp], sb[pp], wl + pp * 1024); sb[pp] += strd[pp]; }
            wl = wl >= K1_W + 4 * K1_WSLOT ? wl - 4 * K1_WSLOT : wl + K1_WSLOT;
        };
        auto ldx = [&]() {
#pragma unroll
            for (int pp = 0; pp < 4; ++pp) dma16((pp & 1) ? vx1 : vx0, xb + pp * 8192, xl + pp * 1024);
            xb += 128;
            xl = xl >= K1_X + 2 * K1_XSLOT ? xl - 2 * K1_XSLOT : xl + K1_XSLOT;
        };
        ldx(); ldw(); ldw(); ldx(); ldw(); ldw(); ldx(); ldw();
        WAIT_VM(28); BARRIER();
        for (int u = 0; u < 8; ++u) {
            if (u < 6) WAIT_VM(23); else if (u == 6) WAIT_VM(14); else WAIT_VM(0);
            BARRIER();
            if (u < 6) ldw();
            if (u < 6) WAIT_VM(19); else if (u == 6) WAIT_VM(5);
            BARRIER();
            if (u < 5) { ldx(); ldw(); }
        }
        BARRIER();
        return;
    }
    const int mh = w >> 1, nh = w & 1;
    if (cg == 3 || (cg == 4 && nh == 0)) k1_compute<0x3FF>(cg, rb, mh, nh, ws);
    else if (cg == 4) k1_compute<0x021>(cg, rb, mh, nh, ws);
    else k1_compute<0>(cg, rb, mh, nh, ws);
}

constexpr int K2_LDS = 131072 + 4096;
constexpr float SAFE_SPAN = 80.f;

DI f32x16 mfma32(u32x4 a, u32x4 b, f32x16 c) {
    return __builtin_amdgcn_mfma_f32_32x32x16_bf16(__builtin_bit_cast(bf16x8, a), __builtin_bit_cast(bf16x8, b), c, 0, 0, 0);
}
DI f32x16 pv(u32x4 v, f32x8 p, f32x16 o) {
    return __builtin_amdgcn_smfmac_f32_32x32x32_bf16(__builtin_bit_cast(bf16x8, v), __builtin_bit_cast(bf16x16, p), o, (int)0xDDDDDDDD, 0, 0);
}
DI f32x8 lo8(f32x16 s) { return __builtin_shufflevector(s, s, 0, 1, 2, 3, 4, 5, 6, 7); }
DI f32x8 hi8(f32x16 s) { return __builtin_shufflevector(s, s, 8, 9, 10, 11, 12, 13, 14, 15); }
DI float swap32_f(float v) { return __shfl_xor(v, 32); }

__global__ __launch_bounds__(512, 2) void k2_attn(char* __restrict__ ws) {
    extern __shared__ __attribute__((aligned(16))) char lds[];
    const int lane = threadIdx.x & 63, w = rfl(threadIdx.x >> 6);
    const int xcd = blockIdx.x & 7, j = blockIdx.x >> 3;
    const int h = xcd * 4 + (j >> 3), qb = j & 7;
    const int kh = w >> 2, qp = w & 3, r = lane & 31, hh = lane >> 5;
    const char* KVg = ws + WS_KV + (size_t)h * 131072;
    const __bf16* Qg = (const __bf16*)(ws + WS_Q) + (size_t)h * 32768 + (qb * 256 + qp * 64) * 16;
    STAMP(1, 0);
    u32x4 qa, qbf; float km;
    asm volatile("global_load_dwordx4 %0, %3, %4\n\tglobal_load_dwordx4 %1, %3, %4 offset:1024\n\tglobal_load_dword %2, %5, %6"
                 : "=&v"(qa), "=&v"(qbf), "=&v"(km) : "v"(r * 32 + hh * 16), "s"(Qg), "v"(r * 4), "s"(ws + WS_KM + h * 128) : "memory");
#pragma unroll
    for (int i = 0; i < 16; ++i) { const int pc = kh * 64 + i * 4 + qp; dma16(lane * 16, KVg + pc * 1024, pc * 1024); }
    asm volatile("s_waitcnt vmcnt(16)" : "+v"(qa), "+v"(qbf), "+v"(km));
#pragma unroll
    for (int o = 1; o < 32; o <<= 1) km = fmaxf(km, __shfl_xor(km, o));
    float na = 0.f, nb = 0.f;
#pragma unroll
    for (int d = 0; d < 4; ++d) {
        na += bf_lo(qa[d]) * bf_lo(qa[d]) + bf_hi(qa[d]) * bf_hi(qa[d]);
        nb += bf_lo(qbf[d]) * bf_lo(qbf[d]) + bf_hi(qbf[d]) * bf_hi(qbf[d]);
    }
    na += swap32_f(na); nb += swap32_f(nb);
    float ma = __builtin_amdgcn_sqrtf(na * km) * 1.001f + 1e-3f, mb = __builtin_amdgcn_sqrtf(nb * km) * 1.001f + 1e-3f;
    const bool fast = __all(2.f * ma <= SAFE_SPAN && 2.f * mb <= SAFE_SPAN);
    f32x16 oa, ob;
#pragma unroll
    for (int i = 0; i < 16; ++i) { oa[i] = 0.f; ob[i] = 0.f; }
    const unsigned cst = (r == 16 || r == 20) ? 0x3f803f80u : 0u;
    u32x4 v0 = {cst, cst, cst, cst}, v1 = v0;
    unsigned adr = kh * 65536 + lane * 16;
    const unsigned long long vmask = 0x0000FFFF0000FFFFull;
    WAIT_VM(12); BARRIER();
    STAMP(1, 1);
    if (fast) {
        f32x16 ca, cb;
#pragma unroll
        for (int i = 0; i < 16; ++i) { ca[i] = -ma; cb[i] = -mb; }
#define EX(n) "v_exp_f32 v" #n ", v" #n "\n\t"
#define RDV(r, off) "s_mov_b64 exec, %[vm]\n\tds_read_b128 " r ", %[adr] offset:" off "\n\ts_mov_b64 exec, -1\n\t"
        int cnt = 8;
        asm volatile(
            "ds_read_b128 v[104:107], %[adr]\n\t"
            "s_waitcnt lgkmcnt(0)\n\t"
            "v_mfma_f32_32x32x16_bf16 v[64:79], v[104:107], v[96:99], v[32:47]\n\t"
            "v_mfma_f32_32x32x16_bf16 v[80:95], v[104:107], v[100:103], v[48:63]\n\t"
            RDV("v[108:111]", "1024") RDV("v[112:115]", "1280")
            "ds_read_b128 v[104:107], %[adr] offset:2048\n\t"
            "s_nop 7\n\t"
            EX(64) EX(65) EX(66) EX(67)
            "1:\n\t"
            ".set kvo, 2048\n\t"
            ".rept 4\n\t"
            EX(68) EX(69) EX(70) EX(71) EX(72) EX(73)
            "s_waitcnt lgkmcnt(2)\n\t"
            "v_smfmac_f32_32x32x32_bf16 v[0:15], v[108:111], v[64:71], %[idx]\n\t"
            EX(74) EX(75) EX(76) EX(77) EX(78) EX(79) EX(80) EX(81)
            "s_waitcnt lgkmcnt(1)\n\t"
            "v_smfmac_f32_32x32x32_bf16 v[0:15], v[112:115], v[72:79], %[idx]\n\t"
            EX(82) EX(83)
            "s_waitcnt lgkmcnt(0)\n\t"
            "v_mfma_f32_32x32x16_bf16 v[64:79], v[104:107], v[96:99], v[32:47]\n\t"
            EX(84) EX(85) EX(86) EX(87) EX(88) EX(89)
            "v_smfmac_f32_32x32x32_bf16 v[16:31], v[108:111], v[80:87], %[idx]\n\t"
            RDV("v[108:111]", "kvo+1024")
            EX(90) EX(91) EX(92) EX(93) EX(94) EX(95) EX(64) EX(65)
            "v_smfmac_f32_32x32x32_bf16 v[16:31], v[112:115], v[88:95], %[idx]\n\t"
            RDV("v[112:115]", "kvo+1280")
            EX(66) EX(67)
            "v_mfma_f32_32x32x16_bf16 v[80:95], v[104:107], v[100:103], v[48:63]\n\t"
            "ds_read_b128 v[104:107], %[adr] offset:kvo+2048\n\t"
            ".set kvo, kvo+2048\n\t"
            ".endr\n\t"
            "v_add_u32 %[adr], 8192, %[adr]\n\t"
            "s_cmp_lg_u32 %[cnt], 8\n\t"
            "s_cbranch_scc1 2f\n\t"
            "s_waitcnt vmcnt(0)\n\t"
            "s_barrier\n\t"
            "2:\n\t"
            "s_sub_u32 %[cnt], %[cnt], 1\n\t"
            "s_cmp_lg_u32 %[cnt], 0\n\t"
            "s_cbranch_scc1 1b\n\t"
            "s_waitcnt lgkmcnt(0)\n\t"
            "s_nop 15\n\t"
            : "+{v[0:15]}"(oa), "+{v[16:31]}"(ob), "+{v[108:111]}"(v0), "+{v[112:115]}"(v1), [adr] "+v"(adr), [cnt] "+s"(cnt)
            : "{v[32:47]}"(ca), "{v[48:63]}"(cb), "{v[96:99]}"(qa), "{v[100:103]}"(qbf), [idx] "v"(0xDDDDDDDDu), [vm] "s"(vmask)
            : "v64", "v65", "v66", "v67", "v68", "v69", "v70", "v71", "v72", "v73", "v74", "v75", "v76", "v77", "v78", "v79", "v80", "v81", "v82", "v83",
              "v84", "v85", "v86", "v87", "v88", "v89", "v90", "v91", "v92", "v93", "v94", "v95", "v104", "v105", "v106", "v107", "scc", "memory");
    } else {
        WAIT_VM(0); BARRIER();
        ma = -INFINITY; mb = -INFINITY;
        for (int t = 0; t < 32; ++t) {
            u32x4 k0;
            asm volatile("ds_read_b128 %0, %3\n\ts_mov_b64 exec, %4\n\tds_read_b128 %1, %3 offset:1024\n\tds_read_b128 %2, %3 offset:1280\n\t"
                         "s_mov_b64 exec, -1\n\ts_waitcnt lgkmcnt(0)" : "=&v"(k0), "+v"(v0), "+v"(v1) : "v"(adr), "s"(vmask));
            f32x16 z;
#pragma unroll
            for (int i = 0; i < 16; ++i) z[i] = 0.f;
            f32x16 sa = mfma32(k0, qa, z), sb = mfma32(k0, qbf, z);
            float xa = sa[0], xb = sb[0];
#pragma unroll
            for (int i = 1; i < 16; ++i) { xa = fmaxf(xa, sa[i]); xb = fmaxf(xb, sb[i]); }
            xa = fmaxf(fmaxf(xa, swap32_f(xa)), ma); xb = fmaxf(fmaxf(xb, swap32_f(xb)), mb);
            const float fa = __builtin_amdgcn_exp2f(ma - xa), fb = __builtin_amdgcn_exp2f(mb - xb);
            ma = xa; mb = xb;
#pragma unroll
            for (int i = 0; i < 16; ++i) { sa[i] = __builtin_amdgcn_exp2f(sa[i] - xa); sb[i] = __builtin_amdgcn_exp2f(sb[i] - xb); oa[i] *= fa; ob[i] *= fb; }
            oa = pv(v0, lo8(sa), oa); oa = pv(v1, hi8(sa), oa); ob = pv(v0, lo8(sb), ob); ob = pv(v1, hi8(sb), ob);
            adr += 2048;
        }
    }
    STAMP(1, 2);
    BARRIER();
    STAMP(1, 3);
    {
        LAS float* sx = (LAS float*)(unsigned)(w * 2560) + lane;
#pragma unroll
        for (int i = 0; i < 9; ++i) sx[i * 64] = kh ? oa[i] : ob[i];
        sx[9 * 64] = kh ? ma : mb;
    }
    BARRIER();
    {
        const LAS float* sx = (const LAS float*)(unsigned)((w ^ 4) * 2560) + lane;
        const float m1 = kh ? mb : ma, m2 = sx[9 * 64], mm = fmaxf(m1, m2);
        const float f1 = __builtin_amdgcn_exp2f(m1 - mm), f2 = __builtin_amdgcn_exp2f(m2 - mm);
        float o[9];
#pragma unroll
        for (int i = 0; i < 9; ++i) o[i] = (kh ? ob[i] : oa[i]) * f1 + sx[i * 64] * f2;
        const float inv = 1.f / o[8];
        unsigned a0 = pk_bf16(o[0] * inv, o[1] * inv), a1 = pk_bf16(o[2] * inv, o[3] * inv);
        unsigned b0 = pk_bf16(o[4] * inv, o[5] * inv), b1 = pk_bf16(o[6] * inv, o[7] * inv);
        auto s0 = __builtin_amdgcn_permlane32_swap(a0, b0, false, false);
        auto s1 = __builtin_amdgcn_permlane32_swap(a1, b1, false, false);
        const int tok = qb * 256 + qp * 64 + kh * 32 + r;
        __bf16* A2 = (__bf16*)(ws + WS_A2);
        *(u32x4*)(A2 + ((size_t)(tok >> 4) * NFT + h) * 256 + (tok & 15) * 16 + hh * 8) = u32x4{s0[0], s1[0], s0[1], s1[1]};
    }
    STAMP(1, 4);
}

constexpr int K3_LDS = 131072;
__global__ __launch_bounds__(512, 2) void k3_out(const __bf16* __restrict__ x, const __bf16* __restrict__ wo, const __bf16* __restrict__ w2,
                                                 const char* __restrict__ ws, __bf16* __restrict__ out) {
    extern __shared__ __attribute__((aligned(16))) char lds[];
    const int lane = threadIdx.x & 63, w = rfl(threadIdx.x >> 6);
    const int xcd = blockIdx.x & 7, j = blockIdx.x >> 3;
    const int rb = xcd * 4 + (j >> 3), cb = j & 7;
    const int fr = lane & 15, g = lane >> 4, q = (lane >> 2) & 3, p = lane & 3;
    const char* A2 = ws + WS_A2 + (size_t)rb * 4 * NFT * 512;
    const unsigned base = w * 16384;
    const unsigned va = lane * 16;
    const unsigned vw0 = (lane >> 3) * 1024 + (((lane & 7) ^ (((lane >> 4) & 1) << 1)) * 16), vw1 = vw0 ^ 64;
    auto load = [&](int ks, unsigned dst) {
        const char* wp = (ks < 16 ? (const char*)wo + (size_t)ks * 32768 : (const char*)w2 + (size_t)(ks - 16) * 32768) + cb * 128;
#pragma unroll
        for (int i = 0; i < 4; ++i) dma16(va, A2 + ((size_t)i * NFT + 2 * ks) * 512, dst + i * 1024);
#pragma unroll
        for (int i = 0; i < 4; ++i) dma16((i & 1) ? vw1 : vw0, wp + i * 8192, dst + 4096 + i * 1024);
    };
    f32x4 acc[4][4];
#pragma unroll
    for (int i = 0; i < 4; ++i)
#pragma unroll
        for (int t = 0; t < 4; ++t) acc[i][t] = f32x4{0.f, 0.f, 0.f, 0.f};
    const unsigned ab = base + (g >> 1) * 512 + fr * 32 + (g & 1) * 16;
    const unsigned wb = base + 4096 + g * 1024 + q * 128 + (((p >> 1) ^ ((((q >> 1) & 1) | ((g & 1) << 1)) << 1)) * 16) + (p & 1) * 8;
    STAMP(2, 0);
    load(w, base); load(w + 8, base + 8192);
    for (int it = 0; it < 9; ++it) {
        const unsigned so = (it & 1) * 8192;
        if (it < 8) WAIT_VM(8); else WAIT_VM(0);
        u32x4 af[4], wf[4];
#pragma unroll
        for (int i = 0; i < 4; ++i) af[i] = lds_ld16(ab + so, i * 1024);
#pragma unroll
        for (int t = 0; t < 4; ++t) wf[t] = lds_tr16((wb + so) ^ (t << 5), 0, 512);
        asm volatile("s_waitcnt lgkmcnt(0)" ::: "memory");
        __builtin_amdgcn_sched_barrier(0);
        if (it < 7) load(w + 8 * (it + 2), base + so);
#pragma unroll
        for (int t = 0; t < 4; ++t)
#pragma unroll
            for (int i = 0; i < 4; ++i) acc[i][t] = mfma16(wf[t], af[i], acc[i][t]);
    }
    STAMP(2, 1);
    BARRIER();
    STAMP(2, 2);
#pragma unroll
    for (int i = 0; i < 4; ++i)
#pragma unroll
        for (int t = 0; t < 4; ++t) *(LAS f32x4*)(base + (i * 4 + t) * 1024 + lane * 16) = acc[i][t];
    BARRIER();
#pragma unroll
    for (int e = 0; e < 2; ++e) {
        const int tile = 2 * w + e, i = tile >> 2, t = tile & 3;
        f32x4 s = *(const LAS f32x4*)(tile * 1024 + lane * 16);
#pragma unroll
        for (int ww = 1; ww < 8; ++ww) s += *(const LAS f32x4*)(ww * 16384 + tile * 1024 + lane * 16);
        const size_t o = (size_t)(rb * 64 + i * 16 + fr) * DM + cb * 64 + t * 16 + 4 * g;
        const u32x2 xv = *(const u32x2*)(x + o);
        *(u32x2*)(out + o) = u32x2{pk_bf16(s[0] + bf_lo(xv[0]), s[1] + bf_hi(xv[0])), pk_bf16(s[2] + bf_lo(xv[1]), s[3] + bf_hi(xv[1]))};
    }
    STAMP(2, 3);
}

extern "C" void kernel_launch(void* const* d_in, const int* in_sizes, int n_in, void* d_out, int out_size, void* d_ws, size_t ws_size,
                              hipStream_t stream) {
    const __bf16* x = (const __bf16*)d_in[0];
    const __bf16* wqkv = (const __bf16*)d_in[1];
    const __bf16* wo = (const __bf16*)d_in[2];
    const __bf16* w1 = (const __bf16*)d_in[4];
    const __bf16* w3 = (const __bf16*)d_in[5];
    const __bf16* w2 = (const __bf16*)d_in[6];
    (void)hipFuncSetAttribute((const void*)k1_proj, hipFuncAttributeMaxDynamicSharedMemorySize, K1_LDS);
    (void)hipFuncSetAttribute((const void*)k2_attn, hipFuncAttributeMaxDynamicSharedMemorySize, K2_LDS);
    (void)hipFuncSetAttribute((const void*)k3_out, hipFuncAttributeMaxDynamicSharedMemorySize, K3_LDS);
    k1_proj<<<256, 512, K1_LDS, stream>>>(x, wqkv, w1, w3, (char*)d_ws);
    k2_attn<<<256, 512, K2_LDS, stream>>>((char*)d_ws);
    k3_out<<<256, 512, K3_LDS, stream>>>(x, wo, w2, (const char*)d_ws, (__bf16*)d_out);
}
```

```cpp
#include <hip/hip_runtime.h>
#include <hip/hip_bf16.h>
#include <stdint.h>
#pragma clang diagnostic ignored "-Wint-to-pointer-cast"

typedef __bf16 bf16x2 __attribute__((ext_vector_type(2)));
typedef __bf16 bf16x8 __attribute__((ext_vector_type(8)));
typedef __bf16 bf16x16 __attribute__((ext_vector_type(16)));
typedef short s16x4 __attribute__((ext_vector_type(4)));
typedef float f32x2 __attribute__((ext_vector_type(2)));
typedef float f32x4 __attribute__((ext_vector_type(4)));
typedef float f32x8 __attribute__((ext_vector_type(8)));
typedef float f32x16 __attribute__((ext_vector_type(16)));
typedef unsigned u32x2 __attribute__((ext_vector_type(2)));
typedef unsigned u32x4 __attribute__((ext_vector_type(4)));
typedef unsigned long long u64;
#define LAS __attribute__((address_space(3)))
#define GAS __attribute__((address_space(1)))
#define DI __device__ __forceinline__
typedef LAS char* lptr;
typedef __amdgpu_buffer_rsrc_t rsrc_t;

constexpr int SEQ = 2048, DM = 512, NH = 32, FF = 1792, NQKV = 1536;
constexpr int NFT = 144;
constexpr float LOG2E = 1.4426950408889634f;
constexpr float QSCALE = 0.25f * LOG2E;
constexpr int SC1 = 16;

constexpr unsigned WS_EPOCH = 0;
constexpr unsigned FS = 128;
constexpr unsigned WS_FQ = 1024;
constexpr unsigned WS_FH = WS_FQ + 256 * FS;
constexpr unsigned WS_FO = WS_FH + 256 * FS;
constexpr unsigned WS_FW = WS_FO + 256 * FS;
constexpr unsigned WS_FQQ = WS_FW + 256 * FS;
constexpr unsigned WS_KM = WS_FQQ + 64 * FS;
constexpr unsigned WS_Q = 1u << 19;
constexpr unsigned WS_KV = WS_Q + NH * SEQ * 32;
constexpr unsigned WS_A2 = WS_KV + NH * SEQ * 64;
constexpr unsigned WS_WT = WS_A2 + 128 * NFT * 512;
constexpr unsigned WS_END = WS_WT + 8 * 288 * 1024;

constexpr int L_WSLOT = 20480, L_XSLOT = 16384, L_X = 5 * L_WSLOT, L_ST = L_X + 3 * L_XSLOT;
constexpr int L_Q = 135168, L_KM = L_Q + 8192, L_MRG = L_Q;
constexpr int L_CTL = 163328, L_TAG = L_CTL, L_CQ = L_CTL + 16, L_CH = L_CTL + 20, L_CO = L_CTL + 24, L_CW = L_CTL + 28, L_KV1 = L_CTL + 32, L_KV2 = L_CTL + 48, L_MF = L_CTL + 64, L_G = L_CTL + 128, L_CQQ = L_CTL + 144;
constexpr int LDS_BYTES = 163840;

#define STAMP(kid, i)
#define SSTAMP(i)
#define SDECL
#define SDUMP(kid, n)

DI unsigned pk_bf16(float lo, float hi) { bf16x2 v = {(__bf16)lo, (__bf16)hi}; return __builtin_bit_cast(unsigned, v); }
DI float bf_lo(unsigned u) { return __uint_as_float(u << 16); }
DI float bf_hi(unsigned u) { return __uint_as_float(u & 0xffff0000u); }
DI float rnd_bf16(float v) { return (float)(__bf16)v; }
DI float dot2(unsigned a, unsigned b, float c) { return __builtin_amdgcn_fdot2_f32_bf16(__builtin_bit_cast(bf16x2, a), __builtin_bit_cast(bf16x2, b), c, false); }
DI int lane_id() { int l; asm volatile("v_mbcnt_lo_u32_b32 %0, -1, 0\n\tv_mbcnt_hi_u32_b32 %0, -1, %0" : "=v"(l)); return l & 63; }
DI int rfl(int x) { return __builtin_amdgcn_readfirstlane(x); }
template <int CTRL> DI float dpp_f(float v) { return __int_as_float(__builtin_amdgcn_update_dpp(0, __float_as_int(v), CTRL, 0xf, 0xf, true)); }
DI float sum32(float v) {
    auto r = __builtin_amdgcn_permlane32_swap(__float_as_uint(v), __float_as_uint(v), false, false);
    return __uint_as_float(r[0]) + __uint_as_float(r[1]);
}
DI float max32(float v) {
    auto r = __builtin_amdgcn_permlane32_swap(__float_as_uint(v), __float_as_uint(v), false, false);
    return fmaxf(__uint_as_float(r[0]), __uint_as_float(r[1]));
}
#define WAIT_VM(n) asm volatile("s_waitcnt vmcnt(" #n ")" ::: "memory")
#define SBAR() __builtin_amdgcn_sched_barrier(0)
#define BAR() asm volatile("s_barrier" ::: "memory")
DI u32x4 lds_ld16(unsigned a, int off) { return *(const LAS u32x4*)((lptr)a + off); }
DI u32x4 lds_tr16(unsigned a, int off, int off2) {
    const u32x2 lo = __builtin_bit_cast(u32x2, __builtin_amdgcn_ds_read_tr16_b64_v4i16((LAS s16x4*)((lptr)a + off)));
    const u32x2 hi = __builtin_bit_cast(u32x2, __builtin_amdgcn_ds_read_tr16_b64_v4i16((LAS s16x4*)((lptr)a + off2)));
    return u32x4{lo[0], lo[1], hi[0], hi[1]};
}
DI f32x4 mfma16(u32x4 a, u32x4 b, f32x4 c) {
    return __builtin_amdgcn_mfma_f32_16x16x32_bf16(__builtin_bit_cast(bf16x8, a), __builtin_bit_cast(bf16x8, b), c, 0, 0, 0);
}
DI f32x16 mfma32(u32x4 a, u32x4 b, f32x16 c) {
    return __builtin_amdgcn_mfma_f32_32x32x16_bf16(__builtin_bit_cast(bf16x8, a), __builtin_bit_cast(bf16x8, b), c, 0, 0, 0);
}
DI f32x16 pv(u32x4 v, f32x8 p, f32x16 o) {
    return __builtin_amdgcn_smfmac_f32_32x32x32_bf16(__builtin_bit_cast(bf16x8, v), __builtin_bit_cast(bf16x16, p), o, (int)0xDDDDDDDD, 0, 0);
}
DI f32x8 lo8(f32x16 s) { return __builtin_shufflevector(s, s, 0, 1, 2, 3, 4, 5, 6, 7); }
DI f32x8 hi8(f32x16 s) { return __builtin_shufflevector(s, s, 8, 9, 10, 11, 12, 13, 14, 15); }
DI rsrc_t mk_rsrc(const void* p, unsigned bytes) { return __builtin_amdgcn_make_buffer_rsrc((void*)p, 0, bytes, 0x00020000); }
DI void st16(u32x4 v, rsrc_t r, unsigned off) { __builtin_amdgcn_raw_buffer_store_b128(v, r, off, 0, SC1); }
DI u64 ld_tag() { return *(volatile LAS u64*)L_TAG; }
DI void set_flag(char* ws, unsigned off, u64 tag) { __hip_atomic_store((GAS u64*)(ws + off), tag, __ATOMIC_RELAXED, __HIP_MEMORY_SCOPE_AGENT); }
#define POLL_SLEEP 4
#define GATE_SLEEP 4
DI void poll_flags(char* ws, unsigned off, int n, u64 tag, int lane) {
    for (int spin = 0; spin < 400000; ++spin) {
        u64 v = tag;
        if (lane < n) v = __hip_atomic_load((GAS u64*)(ws + off), __ATOMIC_RELAXED, __HIP_MEMORY_SCOPE_AGENT);
        if (__all(v == tag)) break;
        __builtin_amdgcn_s_sleep(POLL_SLEEP);
    }
    asm volatile("" ::: "memory");
}
DI void wait_flags(char* ws, unsigned off, int n, u64 tag, int lane, int lw, int gate) {
    if (lw == 0) {
        poll_flags(ws, off, n, tag, lane);
        *(volatile LAS unsigned*)gate = 1u;
    } else {
        for (int spin = 0; spin < 8000000; ++spin) {
            if (rfl(*(volatile LAS unsigned*)gate)) break;
            __builtin_amdgcn_s_sleep(GATE_SLEEP);
        }
    }
    asm volatile("" ::: "memory");
}
DI void arrive(char* ws, int ctr, unsigned total, unsigned off, int lane) {
    if (lane == 0) {
        const unsigned old = __hip_atomic_fetch_add((LAS unsigned*)ctr, 1u, __ATOMIC_RELAXED, __HIP_MEMORY_SCOPE_WORKGROUP);
        if (old == total - 1) set_flag(ws, off, ld_tag());
    }
}
DI void lds_wait4(int addr) {
    for (int spin = 0; spin < 4000000; ++spin) {
        const u32x4 v = *(volatile LAS u32x4*)addr;
        if (rfl(v[0] & v[1] & v[2] & v[3])) break;
        __builtin_amdgcn_s_sleep(1);
    }
    asm volatile("" ::: "memory");
}
DI u32x4 widen(u32x2 a, u32x2 b) {
    auto r0 = __builtin_amdgcn_permlane16_swap(a[0], b[0], false, false);
    auto r1 = __builtin_amdgcn_permlane16_swap(a[1], b[1], false, false);
    return u32x4{r0[0], r1[0], r0[1], r1[1]};
}
DI u32x2 pack4(f32x4 v, float s) { return u32x2{pk_bf16(v[0] * s, v[1] * s), pk_bf16(v[2] * s, v[3] * s)}; }

template <int N> DI void ln_partial(u32x4 (&xr)[N], unsigned dst) {
    float s1 = 0.f, s2 = 0.f, t1 = 0.f, t2 = 0.f;
#pragma unroll
    for (int u = 0; u < N; ++u) {
        s1 = dot2(xr[u][0], 0x3f803f80u, s1); s2 = dot2(xr[u][0], xr[u][0], s2);
        t1 = dot2(xr[u][1], 0x3f803f80u, t1); t2 = dot2(xr[u][1], xr[u][1], t2);
        s1 = dot2(xr[u][2], 0x3f803f80u, s1); s2 = dot2(xr[u][2], xr[u][2], s2);
        t1 = dot2(xr[u][3], 0x3f803f80u, t1); t2 = dot2(xr[u][3], xr[u][3], t2);
    }
    s1 += t1; s2 += t2;
    s1 += dpp_f<0xB1>(s1); s2 += dpp_f<0xB1>(s2); s1 += dpp_f<0x4E>(s1); s2 += dpp_f<0x4E>(s2); s1 += dpp_f<0x141>(s1); s2 += dpp_f<0x141>(s2);
    *(LAS f32x2*)dst = f32x2{s1, s2};
}
DI f32x2 ln_coef(unsigned src) {
    const f32x4 t = *(const LAS f32x4*)src;
    const float mean = (t[0] + t[2]) * (1.f / DM), var = fmaxf((t[1] + t[3]) * (1.f / DM) - mean * mean, 0.f);
    const float ra = __builtin_amdgcn_rcpf(rnd_bf16(__builtin_amdgcn_sqrtf(rnd_bf16(rnd_bf16(var) + 1e-5f))));
    return f32x2{ra, -rnd_bf16(mean) * ra};
}
DI u32x4 ln_apply(u32x4 v, f32x2 c) {
#pragma unroll
    for (int d = 0; d < 4; ++d) v[d] = pk_bf16(fmaf(bf_lo(v[d]), c[0], c[1]), fmaf(bf_hi(v[d]), c[0], c[1]));
    return v;
}

DI const __bf16* group_src(int cg, int a, const __bf16* wqkv, const __bf16* w1, const __bf16* w3) {
    if (a == 4) return wqkv + 512 + 64 * cg;
    if (a >= 2 && cg >= 12) return wqkv + 128 * (cg - 12) + 64 * (a - 2);
    return ((a & 1) ? w3 : w1) + 64 * cg + 1024 * (a >> 1);
}

DI void p1_loader(const __bf16* x, const __bf16* wqkv, const __bf16* w1, const __bf16* w3, const __bf16* wo, const __bf16* w2, char* ws, int cg, int rb, int lw,
                  u32x4 (&wt)[3]) {
    int lane = lane_id();
    SDECL;
    SSTAMP(4);
    u32x4 xr[4][5];
    const int rot = blockIdx.x >> 6;
    const char* xb = (const char*)(x + (size_t)(rb * 128 + lw * 32) * DM);
    const unsigned xvo = (lane >> 3) * 1024 + (lane & 7) * 16;
    auto ldx = [&](int i) {
        const char* p = xb + ((i + rot) & 3) * 8192;
#pragma unroll
        for (int u = 0; u < 5; ++u) asm volatile("global_load_dwordx4 %0, %1, %2 offset:%3" : "=v"(xr[i][u]) : "v"(xvo), "s"(p), "i"(384 + u * 128) : "memory");
    };
    ldx(0); ldx(1);
    SSTAMP(5);
    const int ld2 = cg < 12 ? FF : NQKV, r8 = lane >> 3, krow = (r8 & 3) + 8 * (r8 >> 2) + 16 * (lw & 1) + 4 * (lw >> 1);
    const unsigned cho = ((lane & 7) ^ (((r8 >> 1) & 3) << 1)) * 16;
    unsigned vo0 = krow * FF * 2 + cho, vo2 = krow * ld2 * 2 + cho, vo4 = krow * NQKV * 2 + cho;
    const __bf16* wb[5];
#pragma unroll
    for (int a = 0; a < 5; ++a) wb[a] = group_src(cg, a, wqkv, w1, w3);
    int abl = 0;
    auto ldw = [&](int slot) {
#pragma unroll
        for (int a = 0; a < 5; ++a)
            asm volatile("s_mov_b32 m0, %2\n\ts_nop 0\n\tglobal_load_lds_dwordx4 %0, %1" ::"v"(a < 2 ? vo0 : a < 4 ? vo2 : vo4), "s"(wb[a]), "s"(slot * L_WSLOT + a * 4096 + lw * 1024) : "memory");
        vo0 += 64 * FF; vo2 += 64 * ld2; vo4 += 64 * NQKV;
    };
    asm volatile("" :: "s"(wb[0]), "s"(wb[1]), "s"(wb[2]), "s"(wb[3]), "s"(wb[4]), "v"(vo0), "v"(vo4));
    SSTAMP(6);
#define XWAIT(n, i) asm volatile("s_waitcnt vmcnt(" #n ")" : "+v"(xr[i][0]), "+v"(xr[i][1]), "+v"(xr[i][2]), "+v"(xr[i][3]), "+v"(xr[i][4]))
#define LBAR(n) do { SBAR(); asm volatile("s_waitcnt vmcnt(" #n ") lgkmcnt(0)\n\ts_barrier" ::: "memory"); SBAR(); } while (0)
    const unsigned sta = L_ST + (lw * 32 + (lane >> 3)) * 16;
    auto rowo = [&](int i) { return ((i + rot) & 3) * 128; };
    ldx(2);
    XWAIT(10, 0); ln_partial(xr[0], sta + rowo(0) + 8); SBAR();
    ldx(3);
    XWAIT(10, 1); ln_partial(xr[1], sta + rowo(1) + 8); SBAR();
    ldw(0);
    SSTAMP(0);
    XWAIT(10, 2); ln_partial(xr[2], sta + rowo(2) + 8);
    XWAIT(5, 3); ln_partial(xr[3], sta + rowo(3) + 8);
    SSTAMP(1);
    LBAR(5);
    ldw(1); ldw(2);
    SSTAMP(7);
    f32x2 cf[4];
#pragma unroll
    for (int i = 0; i < 4; ++i) cf[i] = ln_coef(sta + rowo(i));
    auto norm = [&](int u) {
#pragma unroll
        for (int i = 0; i < 4; ++i) { xr[i][u] = ln_apply(xr[i][u], cf[i]); asm volatile("" : "+v"(xr[i][u])); }
    };
    unsigned xw[4];
#pragma unroll
    for (int i = 0; i < 4; ++i) xw[i] = (L_X + lw * 4096 + (lane >> 3) * 128 + (((lane & 7) ^ (lane >> 4)) * 16) + rowo(i) * 8) ^ (((i + rot) & 1) * 64);
    auto putx = [&](int u, int i0, int i1) {
#pragma unroll
        for (int i = i0; i < i1; ++i) *(LAS u32x4*)((lptr)xw[i] + (u % 3) * L_XSLOT) = xr[i][u];
        SBAR();
    };
    norm(0);
    LBAR(10);
    SSTAMP(2);
    LBAR(5); ldw(3); ldw(4); norm(1);
    LBAR(10); ldw(0); norm(2);
    LBAR(10); putx(0, 0, 2); ldw(1); norm(3);
    LBAR(10); putx(0, 2, 4); ldw(2); norm(4);
    LBAR(10); putx(1, 0, 2); ldw(3);
    LBAR(10); putx(1, 2, 4); ldw(4);
    LBAR(10); putx(2, 0, 2); ldw(0);
    LBAR(10); putx(2, 2, 4); ldw(1);
    LBAR(10); putx(3, 0, 2); ldw(2);
    LBAR(10); putx(3, 2, 4); ldw(3);
    LBAR(10); putx(4, 0, 2); ldw(4);
    LBAR(10); putx(4, 2, 4); ldw(0);
    LBAR(10);
    LBAR(5);
    LBAR(0);
    BAR();
    BAR(); SBAR();
    const int xcd = blockIdx.x & 7, j = blockIdx.x >> 3;
    lane = lane_id();
    unsigned short tv[3][8];
#pragma unroll
    for (int f = 0; f < 3; ++f) {
        const int idx = j * 9 + min(lw + 4 * f, 8), ft = idx >> 1, n = xcd * 64 + (idx & 1) * 32 + (lane & 31);
        const unsigned short* src = (const unsigned short*)(ft < 32 ? wo + ft * 16 * DM : w2 + (ft - 32) * 16 * DM) + (lane >> 5) * 8 * DM + n;
#pragma unroll
        for (int d = 0; d < 8; ++d) tv[f][d] = src[d * DM];
    }
#pragma unroll
    for (int f = 0; f < 3; ++f) {
        wt[f] = u32x4{tv[f][0] | ((unsigned)tv[f][1] << 16), tv[f][2] | ((unsigned)tv[f][3] << 16), tv[f][4] | ((unsigned)tv[f][5] << 16), tv[f][6] | ((unsigned)tv[f][7] << 16)};
        asm volatile("" : "+v"(wt[f]));
    }
    SSTAMP(3); SDUMP(1, 8);
}

template <int MASK>
DI void p1_compute(char* ws, int cg, int rb, int mh, int nq) {
    int lane = lane_id(), fr = lane & 15, g = lane >> 4;
    const int q = (lane >> 2) & 3, p = lane & 3;
    unsigned xa = L_X + (mh * 64 + fr) * 128 + ((g ^ (fr >> 1)) * 16);
    unsigned wa = (8 * (g >> 1) + 4 * (g & 1) + q) * 128 + ((nq ^ (2 * (g & 1) + (q >> 1))) * 32) + 8 * p;
    f32x4 acc[4][5];
#pragma unroll
    for (int i = 0; i < 4; ++i)
#pragma unroll
        for (int t = 0; t < 5; ++t) acc[i][t] = f32x4{0.f, 0.f, 0.f, 0.f};
    u32x4 xf[4], xn[4], wf[5];
    auto rdx = [&](u32x4(&d)[4]) {
#pragma unroll
        for (int i = 0; i < 4; ++i) d[i] = lds_ld16(xa, i * 2048);
    };
    STAMP(0, 1);
    BAR();
    STAMP(0, 2);
    rdx(xf);
#pragma unroll
    for (int t = 0; t < 5; ++t) wf[t] = lds_tr16(wa, t * 4096, t * 4096 + 2048);
    int wslot = 0, xslot = 0, stepno = 0;
    auto step = [&](u32x4(&xc)[4], u32x4(&xd)[4], int dx) {
        wa += wslot == 4 ? -4 * L_WSLOT : L_WSLOT;
        wslot = wslot == 4 ? 0 : wslot + 1;
        xa = (xa ^ 64) + dx;
        asm volatile("" : "+v"(xa), "+v"(wa));
        SBAR(); BAR(); SBAR();
#pragma unroll
        for (int t = 0; t < 5; ++t) {
#pragma unroll
            for (int i = 0; i < 4; ++i) {
                if ((MASK >> t) & 1) acc[i][t] = mfma16(xc[i], wf[t], acc[i][t]);
                else acc[i][t] = mfma16(wf[t], xc[i], acc[i][t]);
            }
            SBAR();
            wf[t] = lds_tr16(wa, t * 4096, t * 4096 + 2048);
            if (t == 0) rdx(xd);
            SBAR();
        }
    };
    for (int u = 0; u < 8; ++u) {
        step(xf, xn, 0);
        step(xn, xf, xslot == 2 ? -2 * L_XSLOT : L_XSLOT);
        xslot = xslot == 2 ? 0 : xslot + 1;
    }
    __builtin_amdgcn_s_waitcnt(0xC07F);
    asm volatile("s_waitcnt lgkmcnt(0)\n\ts_barrier" ::: "memory");
    STAMP(0, 3);
    lane = lane_id(); fr = lane & 15; g = lane >> 4;
    const rsrc_t rs = mk_rsrc(ws, WS_END);
    const int tok0 = rb * 128 + mh * 64;
    const unsigned kvb = WS_KV + (tok0 >> 5) * 2048 + (g >> 1) * 512;
    if (MASK) {
#pragma unroll
        for (int i = 0; i < 4; i += 2)
            st16(widen(pack4(acc[i][4], 1.f), pack4(acc[i + 1][4], 1.f)), rs, kvb + (4 * (cg - 8) + nq) * 131072 + i * 1024 + 1024 + (g & 1) * 256 + fr * 16);
    } else {
        const int h = 4 * cg + nq;
        float km = 0.f;
#pragma unroll
        for (int i = 0; i < 4; i += 2) {
            const u32x4 o = widen(pack4(acc[i][4], 1.f), pack4(acc[i + 1][4], 1.f));
            st16(o, rs, kvb + h * 131072 + i * 1024 + (g & 1) * 256 + fr * 16);
            const float n2 = dot2(o[0], o[0], dot2(o[1], o[1], dot2(o[2], o[2], dot2(o[3], o[3], 0.f))));
            km = fmaxf(km, sum32(n2));
        }
        km = fmaxf(km, dpp_f<0xB1>(km)); km = fmaxf(km, dpp_f<0x4E>(km)); km = fmaxf(km, dpp_f<0x141>(km)); km = fmaxf(km, dpp_f<0x140>(km));
        km = fmaxf(km, __shfl_xor(km, 16));
        if (lane == 0) __builtin_amdgcn_raw_buffer_store_b32(__float_as_uint(km), rs, WS_KM + (h * 32 + (tok0 >> 6)) * FS, 0, SC1);
    }
    const bool hasq = MASK && cg >= 12;
    SBAR();
    if (hasq) {
#pragma unroll
        for (int t = 2; t < 4; ++t)
#pragma unroll
            for (int i = 0; i < 4; i += 2)
                st16(widen(pack4(acc[i][t], QSCALE), pack4(acc[i + 1][t], QSCALE)), rs,
                     WS_Q + (8 * (cg - 12) + 4 * (t - 2) + nq) * 65536 + (tok0 + (i + (g & 1)) * 16 + fr) * 32 + (g >> 1) * 16);
    }
    auto glu = [&](int t, u32x4(&hw)[2]) {
        u32x2 hp[4];
#pragma unroll
        for (int i = 0; i < 4; ++i) {
            f32x4 hv;
#pragma unroll
            for (int r = 0; r < 4; ++r) {
                const float gt = acc[i][t][r];
                hv[r] = gt * acc[i][t + 1][r] * __builtin_amdgcn_rcpf(1.f + __builtin_amdgcn_exp2f(-LOG2E * gt));
            }
            hp[i] = pack4(hv, 1.f);
        }
        hw[0] = widen(hp[0], hp[1]); hw[1] = widen(hp[2], hp[3]);
    };
    auto puth = [&](u32x4(&hw)[2], int ft) {
#pragma unroll
        for (int i = 0; i < 2; ++i)
            st16(hw[i], rs, WS_A2 + (((tok0 >> 4) + 2 * i + (g & 1)) * NFT + 32 + ft) * 512 + fr * 32 + (g >> 1) * 16);
    };
    SBAR();
    u32x4 h0[2], h1[2];
    glu(0, h0);
    asm volatile("" : "+v"(h0[0]), "+v"(h0[1]));
    SBAR();
    if (hasq) WAIT_VM(4); else WAIT_VM(0);
    arrive(ws, L_CQ, 8, WS_FQ + (cg * 16 + rb) * FS, lane);
    if (hasq) { WAIT_VM(0); arrive(ws, L_CQQ, 8, WS_FQQ + ((cg - 12) * 16 + rb) * FS, lane); }
    STAMP(0, 4);
    SBAR();
    puth(h0, 4 * cg + nq);
    if (!hasq) { glu(2, h1); puth(h1, 64 + 4 * cg + nq); }
    WAIT_VM(0);
    arrive(ws, L_CH, 8, WS_FH + (rb * 16 + cg) * FS, lane);
    STAMP(0, 5);
}

#define PRIOSW 3
constexpr float SAFE_SPAN = 80.f;

DI void p2_attn(char* ws, int h, int qb, int w) {
    const int lane = lane_id();
    const int kh = w >> 2, qp = w & 3, r = lane & 31, hh = lane >> 5;
    lds_wait4(L_KV1);
    STAMP(2, 0);
    const u32x4 qa = lds_ld16(L_Q + qp * 2048 + r * 32 + hh * 16, 0), qbf = lds_ld16(L_Q + qp * 2048 + r * 32 + hh * 16, 1024);
    float km = *(const LAS float*)(L_KM + r * 4);
    asm volatile("" ::: "memory");
    if (lane == 0) *(volatile LAS unsigned*)(L_MF + w * 4) = 1u;
    km = fmaxf(km, dpp_f<0xB1>(km)); km = fmaxf(km, dpp_f<0x4E>(km)); km = fmaxf(km, dpp_f<0x141>(km)); km = fmaxf(km, dpp_f<0x140>(km));
    km = fmaxf(km, __shfl_xor(km, 16));
    float na = dot2(qa[0], qa[0], dot2(qa[1], qa[1], dot2(qa[2], qa[2], dot2(qa[3], qa[3], 0.f))));
    float nb = dot2(qbf[0], qbf[0], dot2(qbf[1], qbf[1], dot2(qbf[2], qbf[2], dot2(qbf[3], qbf[3], 0.f))));
    na = sum32(na); nb = sum32(nb);
    float ma = __builtin_amdgcn_sqrtf(na * km) * 1.002f + 1e-3f, mb = __builtin_amdgcn_sqrtf(nb * km) * 1.002f + 1e-3f;
    const bool fast = __all(2.f * ma <= SAFE_SPAN && 2.f * mb <= SAFE_SPAN);
    f32x16 oa, ob;
#pragma unroll
    for (int i = 0; i < 16; ++i) { oa[i] = 0.f; ob[i] = 0.f; }
    const unsigned cst = (r == 16 || r == 20) ? 0x3f803f80u : 0u;
    u32x4 v0 = {cst, cst, cst, cst}, v1 = v0;
    unsigned adr = kh * 65536 + lane * 16;
    const u64 vmask = 0x0000FFFF0000FFFFull;
    if (kh) __builtin_amdgcn_s_setprio(1); else __builtin_amdgcn_s_setprio(2);
    if (fast) {
        f32x16 ca, cb;
#pragma unroll
        for (int i = 0; i < 16; ++i) { ca[i] = -ma; cb[i] = -mb; }
#define EX(n) "v_exp_f32 v" #n ", v" #n "\n\t"
#define RDV(r, off) "s_mov_b64 exec, %[vm]\n\tds_read_b128 " r ", %[adr] offset:" off "\n\ts_mov_b64 exec, -1\n\t"
        int cnt = 8, tmp;
        asm volatile(
            "ds_read_b128 v[104:107], %[adr]\n\t"
            "s_waitcnt lgkmcnt(0)\n\t"
            "v_mfma_f32_32x32x16_bf16 v[64:79], v[104:107], v[96:99], v[32:47]\n\t"
            "v_mfma_f32_32x32x16_bf16 v[80:95], v[104:107], v[100:103], v[48:63]\n\t"
            RDV("v[108:111]", "1024") RDV("v[112:115]", "1280")
            "ds_read_b128 v[104:107], %[adr] offset:2048\n\t"
            "s_nop 7\n\t"
            EX(64) EX(65) EX(66) EX(67)
            "1:\n\t"
            ".set kvo, 2048\n\t"
            ".rept 4\n\t"
            EX(68) EX(69) EX(70) EX(71) EX(72) EX(73)
            "s_waitcnt lgkmcnt(2)\n\t"
            "v_smfmac_f32_32x32x32_bf16 v[0:15], v[108:111], v[64:71], %[idx]\n\t"
            EX(74) EX(75) EX(76) EX(77) EX(78) EX(79) EX(80) EX(81)
            "s_waitcnt lgkmcnt(1)\n\t"
            "v_smfmac_f32_32x32x32_bf16 v[0:15], v[112:115], v[72:79], %[idx]\n\t"
            EX(82) EX(83)
            "s_waitcnt lgkmcnt(0)\n\t"
            "v_mfma_f32_32x32x16_bf16 v[64:79], v[104:107], v[96:99], v[32:47]\n\t"
            EX(84) EX(85) EX(86) EX(87) EX(88) EX(89)
            "v_smfmac_f32_32x32x32_bf16 v[16:31], v[108:111], v[80:87], %[idx]\n\t"
            RDV("v[108:111]", "kvo+1024")
            EX(90) EX(91) EX(92) EX(93) EX(94) EX(95) EX(64) EX(65)
            "v_smfmac_f32_32x32x32_bf16 v[16:31], v[112:115], v[88:95], %[idx]\n\t"
            RDV("v[112:115]", "kvo+1280")
            EX(66) EX(67)
            "v_mfma_f32_32x32x16_bf16 v[80:95], v[104:107], v[100:103], v[48:63]\n\t"
            "ds_read_b128 v[104:107], %[adr] offset:kvo+2048\n\t"
            ".set kvo, kvo+2048\n\t"
            ".endr\n\t"
            "v_add_u32 %[adr], 8192, %[adr]\n\t"
            "s_cmp_lg_u32 %[cnt], 8\n\t"
            "s_cbranch_scc1 2f\n\t"
            "s_mov_b32 m0, 4000000\n\t"
            "3:\n\t"
            "ds_read_b128 v[116:119], %[fl]\n\t"
            "s_waitcnt lgkmcnt(0)\n\t"
            "v_and_b32 v116, v116, v117\n\t"
            "v_and_b32 v118, v118, v119\n\t"
            "v_and_b32 v116, v116, v118\n\t"
            "s_nop 1\n\t"
            "v_readfirstlane_b32 %[tmp], v116\n\t"
            "s_sub_u32 m0, m0, 1\n\t"
            "s_cbranch_scc1 2f\n\t"
            "s_cmp_eq_u32 %[tmp], 0\n\t"
            "s_cbranch_scc1 3b\n\t"
            "2:\n\t"
            "s_cmp_lg_u32 %[cnt], %[sw]\n\t"
            "s_cbranch_scc1 4f\n\t"
            "s_setprio 0\n\t"
            "4:\n\t"
            "s_sub_u32 %[cnt], %[cnt], 1\n\t"
            "s_cmp_lg_u32 %[cnt], 0\n\t"
            "s_cbranch_scc1 1b\n\t"
            "s_waitcnt lgkmcnt(0)\n\t"
            "s_nop 15\n\t"
            : "+{v[0:15]}"(oa), "+{v[16:31]}"(ob), "+{v[108:111]}"(v0), "+{v[112:115]}"(v1), [adr] "+v"(adr), [cnt] "+s"(cnt), [tmp] "=&s"(tmp)
            : "{v[32:47]}"(ca), "{v[48:63]}"(cb), "{v[96:99]}"(qa), "{v[100:103]}"(qbf), [idx] "v"(0xDDDDDDDDu), [vm] "s"(vmask), [fl] "v"(L_KV2)
              , [sw] "s"(kh ? -1 : PRIOSW)

            : "v64", "v65", "v66", "v67", "v68", "v69", "v70", "v71", "v72", "v73", "v74", "v75", "v76", "v77", "v78", "v79", "v80", "v81", "v82", "v83",
              "v84", "v85", "v86", "v87", "v88", "v89", "v90", "v91", "v92", "v93", "v94", "v95", "v104", "v105", "v106", "v107", "v116", "v117", "v118",
              "v119", "scc", "memory");
    } else {
        lds_wait4(L_KV2);
        ma = -INFINITY; mb = -INFINITY;
        for (int t = 0; t < 32; ++t) {
            u32x4 k0;
            asm volatile("ds_read_b128 %0, %3\n\ts_mov_b64 exec, %4\n\tds_read_b128 %1, %3 offset:1024\n\tds_read_b128 %2, %3 offset:1280\n\t"
                         "s_mov_b64 exec, -1\n\ts_waitcnt lgkmcnt(0)" : "=&v"(k0), "+v"(v0), "+v"(v1) : "v"(adr), "s"(vmask));
            f32x16 z;
#pragma unroll
            for (int i = 0; i < 16; ++i) z[i] = 0.f;
            f32x16 sa = mfma32(k0, qa, z), sb = mfma32(k0, qbf, z);
            float xa = sa[0], xb = sb[0];
#pragma unroll
            for (int i = 1; i < 16; ++i) { xa = fmaxf(xa, sa[i]); xb = fmaxf(xb, sb[i]); }
            xa = fmaxf(max32(xa), ma); xb = fmaxf(max32(xb), mb);
            const float fa = __builtin_amdgcn_exp2f(ma - xa), fb = __builtin_amdgcn_exp2f(mb - xb);
            ma = xa; mb = xb;
#pragma unroll
            for (int i = 0; i < 16; ++i) { sa[i] = __builtin_amdgcn_exp2f(sa[i] - xa); sb[i] = __builtin_amdgcn_exp2f(sb[i] - xb); oa[i] *= fa; ob[i] *= fb; }
            oa = pv(v0, lo8(sa), oa); oa = pv(v1, hi8(sa), oa); ob = pv(v0, lo8(sb), ob); ob = pv(v1, hi8(sb), ob);
            adr += 2048;
        }
    }
    STAMP(2, 1);
    {
        lds_wait4(L_MF); lds_wait4(L_MF + 16);
        LAS float* sx = (LAS float*)(L_MRG + w * 2560) + lane;
#pragma unroll
        for (int i = 0; i < 9; ++i) sx[i * 64] = kh ? oa[i] : ob[i];
        sx[9 * 64] = kh ? ma : mb;
        asm volatile("s_waitcnt lgkmcnt(0)" ::: "memory");
        if (lane == 0) *(volatile LAS unsigned*)(L_MF + 32 + w * 4) = 1u;
        for (int spin = 0; spin < 4000000; ++spin) {
            if (rfl(*(volatile LAS unsigned*)(L_MF + 32 + (w ^ 4) * 4))) break;
            __builtin_amdgcn_s_sleep(1);
        }
        asm volatile("" ::: "memory");
    }
    {
        const LAS float* sx = (const LAS float*)(L_MRG + (w ^ 4) * 2560) + lane;
        const float m1 = kh ? mb : ma, m2 = sx[9 * 64], mm = fmaxf(m1, m2);
        const float f1 = __builtin_amdgcn_exp2f(m1 - mm), f2 = __builtin_amdgcn_exp2f(m2 - mm);
        float o[9];
#pragma unroll
        for (int i = 0; i < 9; ++i) o[i] = (kh ? ob[i] : oa[i]) * f1 + sx[i * 64] * f2;
        const float inv = 1.f / o[8];
        unsigned a0 = pk_bf16(o[0] * inv, o[1] * inv), a1 = pk_bf16(o[2] * inv, o[3] * inv);
        unsigned b0 = pk_bf16(o[4] * inv, o[5] * inv), b1 = pk_bf16(o[6] * inv, o[7] * inv);
        auto s0 = __builtin_amdgcn_permlane32_swap(a0, b0, false, false);
        auto s1 = __builtin_amdgcn_permlane32_swap(a1, b1, false, false);
        const int tok = qb * 256 + qp * 64 + kh * 32 + r;
        st16(u32x4{s0[0], s1[0], s0[1], s1[1]}, mk_rsrc(ws, WS_END), WS_A2 + ((tok >> 4) * NFT + h) * 512 + (tok & 15) * 32 + hh * 16);
    }
    WAIT_VM(0);
    arrive(ws, L_CO, 8, WS_FO + (qb * 32 + h) * FS, lane);
    STAMP(2, 2);
}

DI void p2_gemm(const __bf16* x, char* ws, __bf16* out, int h, int qb, int lw, const u32x4 (&wt)[3]) {
    const int lane = lane_id();
    const u64 tag = ld_tag();
    __builtin_amdgcn_s_setprio(3);
    auto dma = [&](const char* base, unsigned vo, unsigned ldsa) {
        asm volatile("s_mov_b32 m0, %2\n\ts_nop 0\n\tglobal_load_lds_dwordx4 %0, %1 sc1" ::"v"(vo), "s"(base), "s"(ldsa) : "memory");
    };
    if (lw == 1) {
        poll_flags(ws, WS_FQQ + ((h >> 3) * 16 + 2 * qb + (lane & 1)) * FS, 2, tag, lane);
#pragma unroll
        for (int i = 0; i < 8; ++i) dma(ws + WS_Q + h * 65536 + qb * 8192, lane * 16 + i * 1024, L_Q + i * 1024);
    }
    wait_flags(ws, WS_FQ + (((h >> 2) + (lane & 16) / 2) * 16 + (lane & 15)) * FS, 32, tag, lane, lw, L_G);
    STAMP(3, 0);
    const int xcd = blockIdx.x & 7, j = blockIdx.x >> 3, rb4 = xcd * 4 + (j >> 3), cb = j & 7;
    const rsrc_t rs = mk_rsrc(ws, WS_END);
    {
        const char* kvg = ws + WS_KV + h * 131072;
        unsigned vo = (lw + 8 * qb) * 1024 + lane * 16;
        auto kv4 = [&](int n) {
            dma(kvg, vo, (4 * n + lw) * 1024); dma(kvg + 65536, vo, (64 + 4 * n + lw) * 1024);
            vo = (vo + 4096) & 65535;
        };
        if (lw == 2) asm volatile("s_mov_b32 m0, %2\n\ts_nop 0\n\tglobal_load_lds_dword %0, %1 sc1" ::"v"((lane & 31) * FS), "s"(ws + WS_KM + h * 32 * FS), "s"(L_KM) : "memory");
        kv4(0); kv4(1); kv4(2); kv4(3);
        kv4(4); kv4(5);
        WAIT_VM(4);
        if (lane == 0) *(volatile LAS unsigned*)(L_KV1 + lw * 4) = 1u;
#pragma unroll
        for (int n = 6; n < 16; ++n) kv4(n);
    }
#pragma unroll
    for (int f = 0; f < 3; ++f)
        if (lw + 4 * f < 9) st16(wt[f], rs, WS_WT + (xcd * 288 + j * 9 + lw + 4 * f) * 1024 + lane * 16);
    if (lw == 0) WAIT_VM(3); else WAIT_VM(2);
    if (lane == 0) *(volatile LAS unsigned*)(L_KV2 + lw * 4) = 1u;
    WAIT_VM(0);
    arrive(ws, L_CW, 4, WS_FW + (xcd * 32 + j) * FS, lane);
    STAMP(3, 1);
    const int r = lane & 31, hh = lane >> 5;
    const unsigned va = WS_A2 + (rb4 * 4 + (r >> 4)) * NFT * 512 + (r & 15) * 32 + hh * 16, vw = WS_WT + cb * 288 * 1024 + lane * 16;
    f32x16 acc[2][2];
#pragma unroll
    for (int i = 0; i < 16; ++i) { acc[0][0][i] = 0.f; acc[0][1][i] = 0.f; acc[1][0][i] = 0.f; acc[1][1][i] = 0.f; }
    u32x4 a0[4], w0[4], a1[4], w1[4];
    auto ld = [&](u32x4(&a)[4], u32x4(&w)[4], int ks) {
#pragma unroll
        for (int e = 0; e < 4; ++e) {
            w[e] = __builtin_amdgcn_raw_buffer_load_b128(rs, vw + e * 1024, ks * 4096, SC1);
            a[e] = __builtin_amdgcn_raw_buffer_load_b128(rs, va + (e & 1) * 2 * NFT * 512 + (e >> 1) * 512, ks * 1024, SC1);
        }
    };
#define PACE 1
    auto mm = [&](u32x4(&a)[4], u32x4(&w)[4], bool pace) {
#pragma unroll
        for (int e = 0; e < 8; ++e) {
            acc[e & 1][(e >> 1) & 1] = mfma32(w[(e >> 2) * 2 + ((e >> 1) & 1)], a[(e >> 2) * 2 + (e & 1)], acc[e & 1][(e >> 1) & 1]);
            if (pace) { SBAR(); __builtin_amdgcn_s_sleep(PACE); SBAR(); }
        }
    };
    wait_flags(ws, lane < 32 ? WS_FW + (cb * 32 + lane) * FS : WS_FH + ((rb4 >> 1) * 16 + (lane & 15)) * FS, 48, tag, lane, lw, L_G + 4);
    STAMP(3, 2);
    {
    ld(a0, w0, 16 + lw); ld(a1, w1, 20 + lw);
    for (int it = 0; it < 6; ++it) {
        mm(a0, w0, true); SBAR(); ld(a0, w0, 24 + lw + 8 * it); SBAR();
        mm(a1, w1, true); SBAR(); ld(a1, w1, 28 + lw + 8 * it); SBAR();
    }
    mm(a0, w0, true); mm(a1, w1, true);
    }
    STAMP(3, 3);
    const size_t o = (size_t)(rb4 * 64 + (lw >> 1) * 32 + r) * DM + cb * 64 + (lw & 1) * 32 + 4 * hh;
    u32x2 xv[4];
#pragma unroll
    for (int q = 0; q < 4; ++q) xv[q] = *(const u32x2*)(x + o + q * 8);
    wait_flags(ws, WS_FO + ((rb4 >> 2) * 32 + lane) * FS, 32, tag, lane, lw, L_G + 8);
    STAMP(3, 4);
    ld(a0, w0, lw); ld(a1, w1, 4 + lw);
    mm(a0, w0, false); SBAR(); ld(a0, w0, 8 + lw); SBAR();
    mm(a1, w1, false); SBAR(); ld(a1, w1, 12 + lw); SBAR();
    mm(a0, w0, false); mm(a1, w1, false);
    STAMP(3, 5);
    BAR();
#pragma unroll
    for (int e = 0; e < 4; ++e)
#pragma unroll
        for (int q = 0; q < 4; ++q)
            *(LAS f32x4*)(lw * 16384 + (e * 4 + q) * 1024 + lane * 16) = f32x4{acc[e >> 1][e & 1][4 * q], acc[e >> 1][e & 1][4 * q + 1], acc[e >> 1][e & 1][4 * q + 2], acc[e >> 1][e & 1][4 * q + 3]};
    __builtin_amdgcn_s_waitcnt(0xC07F);
    BAR();
#pragma unroll
    for (int q = 0; q < 4; ++q) {
        f32x4 s = *(const LAS f32x4*)((lw * 4 + q) * 1024 + lane * 16);
#pragma unroll
        for (int ww = 1; ww < 4; ++ww) s += *(const LAS f32x4*)(ww * 16384 + (lw * 4 + q) * 1024 + lane * 16);
        *(u32x2*)(out + o + q * 8) = u32x2{pk_bf16(s[0] + bf_lo(xv[q][0]), s[1] + bf_hi(xv[q][0])), pk_bf16(s[2] + bf_lo(xv[q][1]), s[3] + bf_hi(xv[q][1]))};
    }
    if (blockIdx.x == 0 && lw == 0 && lane == 0) set_flag(ws, WS_EPOCH, tag);
    STAMP(3, 6);
}

__global__ __launch_bounds__(768, 3) void fused_block(const __bf16* __restrict__ x, const __bf16* __restrict__ wqkv, const __bf16* __restrict__ wo,
                                                      const __bf16* __restrict__ w1, const __bf16* __restrict__ w3, const __bf16* __restrict__ w2,
                                                      char* __restrict__ ws, __bf16* __restrict__ out) {
    const int w = rfl(threadIdx.x >> 6);
    const int xcd = blockIdx.x & 7, j = blockIdx.x >> 3;
    const int cg = (xcd >> 1) * 4 + (j >> 3), rb = (xcd & 1) * 8 + (j & 7);
    const int h = xcd * 4 + (j >> 3), qb = j & 7;
    if (__builtin_expect(w >= 8, 1)) {
        u32x4 wt[3];
        p1_loader(x, wqkv, w1, w3, wo, w2, ws, cg, rb, w - 8, wt);
        p2_gemm(x, ws, out, h, qb, w - 8, wt);
        return;
    }
    STAMP(0, 0);
    unsigned warm0;
    {
        const int lane = lane_id();
        u32x4 xr[2][3];
        const char* xb = (const char*)(x + (size_t)(rb * 128 + w * 16) * DM);
        const unsigned vo = (lane >> 3) * 1024 + (lane & 7) * 16;
#pragma unroll
        for (int i = 0; i < 2; ++i)
#pragma unroll
            for (int u = 0; u < 3; ++u)
                asm volatile("global_load_dwordx4 %0, %1, %2 offset:%3" : "=v"(xr[i][u]) : "v"(vo), "s"(xb + i * 8192), "i"(u * 128) : "memory");
        if (w == 7) {
            u64 e;
            asm volatile("s_load_dwordx2 %0, %1, 0x0\n\ts_waitcnt lgkmcnt(0)" : "=s"(e) : "s"(ws + WS_EPOCH) : "memory");
            *(LAS unsigned*)(L_CTL + 16 + lane * 4) = 0u;
            *(LAS u64*)L_TAG = e + 1;
        }
        const unsigned sta = L_ST + (w * 16 + (lane >> 3)) * 16;
        asm volatile("s_waitcnt vmcnt(3)" : "+v"(xr[0][0]), "+v"(xr[0][1]), "+v"(xr[0][2]));
        ln_partial(xr[0], sta);
        asm volatile("s_waitcnt vmcnt(0)" : "+v"(xr[1][0]), "+v"(xr[1][1]), "+v"(xr[1][2]));
        STAMP(0, 6);
        const __bf16* src = group_src(cg, min(w, 4), wqkv, w1, w3);
        asm volatile("global_load_dword %0, %1, %2" : "=v"(warm0) : "v"(((rb & 7) * 64 + lane) * (w < 2 || (w < 4 && cg < 12) ? FF * 2 : NQKV * 2)), "s"(src));
        ln_partial(xr[1], sta + 128);
        __builtin_amdgcn_s_waitcnt(0xC07F);
        BAR();
        STAMP(0, 7);
        const unsigned xw = L_X + w * 2048 + (lane >> 3) * 128 + (((lane & 7) ^ (lane >> 4)) * 16);
#pragma unroll
        for (int i = 0; i < 2; ++i) {
            const f32x2 c = ln_coef(sta + i * 128);
#pragma unroll
            for (int u = 0; u < 3; ++u) *(LAS u32x4*)((lptr)(xw ^ (i * 64)) + u * L_XSLOT + i * 1024) = ln_apply(xr[i][u], c);
        }
        __builtin_amdgcn_s_waitcnt(0xC07F);
    }
    if (cg >= 8) p1_compute<0x10>(ws, cg, rb, w >> 2, w & 3);
    else p1_compute<0>(ws, cg, rb, w >> 2, w & 3);
    asm volatile("" ::"v"(warm0));
    p2_attn(ws, h, qb, w);
}

extern "C" void kernel_launch(void* const* d_in, const int* in_sizes, int n_in, void* d_out, int out_size, void* d_ws, size_t ws_size,
                              hipStream_t stream) {
    (void)hipFuncSetAttribute((const void*)fused_block, hipFuncAttributeMaxDynamicSharedMemorySize, LDS_BYTES);
    fused_block<<<256, 768, LDS_BYTES, stream>>>((const __bf16*)d_in[0], (const __bf16*)d_in[1], (const __bf16*)d_in[2], (const __bf16*)d_in[4],
                                                 (const __bf16*)d_in[5], (const __bf16*)d_in[6], (char*)d_ws, (__bf16*)d_out);
}
```

```cpp
#include <hip/hip_runtime.h>
#include <hip/hip_bf16.h>
#include <stdint.h>
#pragma clang diagnostic ignored "-Wint-to-pointer-cast"

typedef __bf16 bf16x2 __attribute__((ext_vector_type(2)));
typedef __bf16 bf16x8 __attribute__((ext_vector_type(8)));
typedef __bf16 bf16x16 __attribute__((ext_vector_type(16)));
typedef short s16x4 __attribute__((ext_vector_type(4)));
typedef float f32x2 __attribute__((ext_vector_type(2)));
typedef float f32x4 __attribute__((ext_vector_type(4)));
typedef float f32x8 __attribute__((ext_vector_type(8)));
typedef float f32x16 __attribute__((ext_vector_type(16)));
typedef unsigned u32x2 __attribute__((ext_vector_type(2)));
typedef unsigned u32x4 __attribute__((ext_vector_type(4)));
typedef unsigned long long u64;
#define LAS __attribute__((address_space(3)))
#define GAS __attribute__((address_space(1)))
#define DI __device__ __forceinline__
typedef LAS char* lptr;
typedef __amdgpu_buffer_rsrc_t rsrc_t;

constexpr int SEQ = 2048, DM = 512, NH = 32, FF = 1792, NQKV = 1536;
constexpr int NFT = 144;
constexpr float LOG2E = 1.4426950408889634f;
constexpr float QSCALE = 0.25f * LOG2E;
#define KVN 2
#define QN 3
constexpr int SC1 = 16;

constexpr unsigned WS_EPOCH = 0;
constexpr unsigned FS = 128;
constexpr unsigned WS_FQ = 1024;
constexpr unsigned WS_FH = WS_FQ + 256 * FS;
constexpr unsigned WS_FO = WS_FH + 256 * FS;
constexpr unsigned WS_FW = WS_FO + 256 * FS;
constexpr unsigned WS_FQQ = WS_FW + 256 * FS;
constexpr unsigned WS_KM = WS_FQQ + 64 * FS;
constexpr unsigned WS_Q = 1u << 19;
constexpr unsigned WS_KV = WS_Q + NH * SEQ * 32;
constexpr unsigned WS_A2 = WS_KV + NH * SEQ * 64;
constexpr unsigned WS_WT = WS_A2 + 128 * NFT * 512;
constexpr unsigned WS_END = WS_WT + 8 * 288 * 1024;

constexpr int L_WSLOT = 24576, L_XSLOT = 16384, L_X = 4 * L_WSLOT, L_ST = L_X + 3 * L_XSLOT, L_SD = L_ST + 2048, L_KM = L_SD + 512, L_VT = L_KM + 512;
constexpr int L_Q = L_VT, L_MRG = 135168;
constexpr int P2W(int n, int grp) {
    return (n == 2 || n == 7) ? 16384 + grp * 24576 : (n == 0 ? 0 : (n == 1 || n == 6) ? 24576 : n == 3 ? 49152 : n == 4 ? 65536 : 81920) + grp * 8192;
}
constexpr int L_CTL = 163328, L_TAG = L_CTL, L_CQ = L_CTL + 16,  L_CO = L_CTL + 24, L_CW = L_CTL + 28, L_KV1 = L_CTL + 32, L_KV2 = L_CTL + 48, L_MF = L_CTL + 64, L_G = L_CTL + 128, L_CQQ = L_CTL + 144, L_CD = L_CTL + 148, L_CH = L_CTL + 152, L_AD = L_CTL + 156;
constexpr int LDS_BYTES = 163840;

#define STAMP(kid, i)
#define SARG
#define SPASS
#define SDECL
#define SDUMP(k0)

DI unsigned pk_bf16(float lo, float hi) { bf16x2 v = {(__bf16)lo, (__bf16)hi}; return __builtin_bit_cast(unsigned, v); }
DI float bf_lo(unsigned u) { return __uint_as_float(u << 16); }
DI float bf_hi(unsigned u) { return __uint_as_float(u & 0xffff0000u); }
DI float rnd_bf16(float v) { return (float)(__bf16)v; }
DI float dot2(unsigned a, unsigned b, float c) { return __builtin_amdgcn_fdot2_f32_bf16(__builtin_bit_cast(bf16x2, a), __builtin_bit_cast(bf16x2, b), c, false); }
DI int lane_id() { int l; asm volatile("v_mbcnt_lo_u32_b32 %0, -1, 0\n\tv_mbcnt_hi_u32_b32 %0, -1, %0" : "=v"(l)); return l & 63; }
DI int rfl(int x) { return __builtin_amdgcn_readfirstlane(x); }
template <int CTRL> DI float dpp_f(float v) { return __int_as_float(__builtin_amdgcn_update_dpp(0, __float_as_int(v), CTRL, 0xf, 0xf, true)); }
DI float sum32(float v) {
    auto r = __builtin_amdgcn_permlane32_swap(__float_as_uint(v), __float_as_uint(v), false, false);
    return __uint_as_float(r[0]) + __uint_as_float(r[1]);
}
DI float max32(float v) {
    auto r = __builtin_amdgcn_permlane32_swap(__float_as_uint(v), __float_as_uint(v), false, false);
    return fmaxf(__uint_as_float(r[0]), __uint_as_float(r[1]));
}
DI float max16(float v) {
    auto r = __builtin_amdgcn_permlane16_swap(__float_as_uint(v), __float_as_uint(v), false, false);
    return fmaxf(__uint_as_float(r[0]), __uint_as_float(r[1]));
}
#define WAIT_VM(n) asm volatile("s_waitcnt vmcnt(" #n ")" ::: "memory")
#define SBAR() __builtin_amdgcn_sched_barrier(0)
#define BAR() asm volatile("s_barrier" ::: "memory")
DI u32x4 lds_ld16(unsigned a, int off) { return *(const LAS u32x4*)((lptr)a + off); }
DI u32x4 lds_tr16(unsigned a, int off, int off2) {
    const u32x2 lo = __builtin_bit_cast(u32x2, __builtin_amdgcn_ds_read_tr16_b64_v4i16((LAS s16x4*)((lptr)a + off)));
    const u32x2 hi = __builtin_bit_cast(u32x2, __builtin_amdgcn_ds_read_tr16_b64_v4i16((LAS s16x4*)((lptr)a + off2)));
    return u32x4{lo[0], lo[1], hi[0], hi[1]};
}
DI f32x4 mfma16(u32x4 a, u32x4 b, f32x4 c) {
    return __builtin_amdgcn_mfma_f32_16x16x32_bf16(__builtin_bit_cast(bf16x8, a), __builtin_bit_cast(bf16x8, b), c, 0, 0, 0);
}
DI f32x16 mfma32(u32x4 a, u32x4 b, f32x16 c) {
    return __builtin_amdgcn_mfma_f32_32x32x16_bf16(__builtin_bit_cast(bf16x8, a), __builtin_bit_cast(bf16x8, b), c, 0, 0, 0);
}
DI f32x16 pv(u32x4 v, f32x8 p, f32x16 o) {
    return __builtin_amdgcn_smfmac_f32_32x32x32_bf16(__builtin_bit_cast(bf16x8, v), __builtin_bit_cast(bf16x16, p), o, (int)0xDDDDDDDD, 0, 0);
}
DI f32x8 lo8(f32x16 s) { return __builtin_shufflevector(s, s, 0, 1, 2, 3, 4, 5, 6, 7); }
DI f32x8 hi8(f32x16 s) { return __builtin_shufflevector(s, s, 8, 9, 10, 11, 12, 13, 14, 15); }
DI rsrc_t mk_rsrc(const void* p, unsigned bytes) { return __builtin_amdgcn_make_buffer_rsrc((void*)p, 0, bytes, 0x00020000); }
DI void st16(u32x4 v, rsrc_t r, unsigned off) { __builtin_amdgcn_raw_buffer_store_b128(v, r, off, 0, SC1); }
DI u64 ld_tag() { return *(volatile LAS u64*)L_TAG; }
DI void set_flag(char* ws, unsigned off, u64 tag) { __hip_atomic_store((GAS u64*)(ws + off), tag, __ATOMIC_RELAXED, __HIP_MEMORY_SCOPE_AGENT); }
#define POLL_SLEEP 4
#define GATE_SLEEP 4
DI void poll_flags(char* ws, unsigned off, int n, u64 tag, int lane) {
    for (int spin = 0; spin < 400000; ++spin) {
        u64 v = tag;
        if (lane < n) v = __hip_atomic_load((GAS u64*)(ws + off), __ATOMIC_RELAXED, __HIP_MEMORY_SCOPE_AGENT);
        if (__all(v == tag)) break;
        __builtin_amdgcn_s_sleep(POLL_SLEEP);
    }
    asm volatile("" ::: "memory");
}
DI void wait_flags(char* ws, unsigned off, int n, u64 tag, int lane, int lw, int gate) {
    if (lw == 0) {
        poll_flags(ws, off, n, tag, lane);
        *(volatile LAS unsigned*)gate = 1u;
    } else {
        for (int spin = 0; spin < 8000000; ++spin) {
            if (rfl(*(volatile LAS unsigned*)gate)) break;
            __builtin_amdgcn_s_sleep(GATE_SLEEP);
        }
    }
    asm volatile("" ::: "memory");
}
DI void arrive(char* ws, int ctr, unsigned total, unsigned off, int lane) {
    if (lane == 0) {
        const unsigned old = __hip_atomic_fetch_add((LAS unsigned*)ctr, 1u, __ATOMIC_RELAXED, __HIP_MEMORY_SCOPE_WORKGROUP);
        if (old == total - 1) set_flag(ws, off, ld_tag());
    }
}
DI void lds_inc(int addr) { asm volatile("s_mov_b64 exec, 1\n\tds_add_u32 %0, %1\n\ts_mov_b64 exec, -1" ::"v"(addr), "v"(1) : "memory"); }
DI void lds_wait_eq(int addr, unsigned n) {
    for (int spin = 0; spin < 8000000; ++spin) {
        if ((unsigned)rfl(*(volatile LAS unsigned*)addr) == n) break;
        __builtin_amdgcn_s_sleep(1);
    }
    asm volatile("" ::: "memory");
}
DI void lds_wait_ge(int addr, unsigned n) {
    for (int spin = 0; spin < 1000000; ++spin) {
        if ((unsigned)rfl(*(volatile LAS unsigned*)addr) >= n) break;
        __builtin_amdgcn_s_sleep(8);
    }
    asm volatile("" ::: "memory");
}
DI void lds_wait4(int addr) {
    for (int spin = 0; spin < 4000000; ++spin) {
        const u32x4 v = *(volatile LAS u32x4*)addr;
        if (rfl(v[0] & v[1] & v[2] & v[3])) break;
        __builtin_amdgcn_s_sleep(1);
    }
    asm volatile("" ::: "memory");
}
DI u32x4 widen(u32x2 a, u32x2 b) {
    auto r0 = __builtin_amdgcn_permlane16_swap(a[0], b[0], false, false);
    auto r1 = __builtin_amdgcn_permlane16_swap(a[1], b[1], false, false);
    return u32x4{r0[0], r1[0], r0[1], r1[1]};
}
DI u32x2 pack4(f32x4 v, float s) { return u32x2{pk_bf16(v[0] * s, v[1] * s), pk_bf16(v[2] * s, v[3] * s)}; }

template <int N> DI void ln_partial(u32x4 (&xr)[N], unsigned dst) {
    float s1 = 0.f, s2 = 0.f, t1 = 0.f, t2 = 0.f;
#pragma unroll
    for (int u = 0; u < N; ++u) {
        s1 = dot2(xr[u][0], 0x3f803f80u, s1); s2 = dot2(xr[u][0], xr[u][0], s2);
        t1 = dot2(xr[u][1], 0x3f803f80u, t1); t2 = dot2(xr[u][1], xr[u][1], t2);
        s1 = dot2(xr[u][2], 0x3f803f80u, s1); s2 = dot2(xr[u][2], xr[u][2], s2);
        t1 = dot2(xr[u][3], 0x3f803f80u, t1); t2 = dot2(xr[u][3], xr[u][3], t2);
    }
    s1 += t1; s2 += t2;
    s1 += dpp_f<0xB1>(s1); s2 += dpp_f<0xB1>(s2); s1 += dpp_f<0x4E>(s1); s2 += dpp_f<0x4E>(s2); s1 += dpp_f<0x141>(s1); s2 += dpp_f<0x141>(s2);
    *(LAS f32x2*)dst = f32x2{s1, s2};
}
DI float dot2v(unsigned a, unsigned b, float c) { float d; asm("v_dot2_f32_bf16 %0, %1, %2, %3" : "=v"(d) : "v"(a), "s"(b), "v"(c)); return d; }
DI float ln_negmean(unsigned src) {
    float nm = -rnd_bf16((*(const LAS float*)src + *(const LAS float*)(src + 8)) * (1.f / DM));
    asm volatile("s_nop 1" : "+v"(nm));
    return nm;
}
DI float ln_std(unsigned src) {
    const f32x4 t = *(const LAS f32x4*)src;
    const float mean = (t[0] + t[2]) * (1.f / DM), var = fmaxf((t[1] + t[3]) * (1.f / DM) - mean * mean, 0.f);
    return rnd_bf16(__builtin_amdgcn_sqrtf(rnd_bf16(rnd_bf16(var) + 1e-5f)));
}
DI void ln_center2(u32x4& v, float nv, u32x4& w, float nw) {
    float a[8], b[8];
#pragma unroll
    for (int d = 0; d < 4; ++d) { a[2 * d] = dot2v(v[d], 0x00003f80u, nv); a[2 * d + 1] = dot2v(v[d], 0x3f800000u, nv); }
    asm volatile("" : "+v"(a[0]), "+v"(a[1]), "+v"(a[2]), "+v"(a[3]), "+v"(a[4]), "+v"(a[5]), "+v"(a[6]), "+v"(a[7]));
    SBAR();
#pragma unroll
    for (int d = 0; d < 4; ++d) { b[2 * d] = dot2v(w[d], 0x00003f80u, nw); b[2 * d + 1] = dot2v(w[d], 0x3f800000u, nw); }
    asm volatile("" : "+v"(b[0]), "+v"(b[1]), "+v"(b[2]), "+v"(b[3]), "+v"(b[4]), "+v"(b[5]), "+v"(b[6]), "+v"(b[7]));
    SBAR();
#pragma unroll
    for (int d = 0; d < 4; ++d) v[d] = pk_bf16(a[2 * d], a[2 * d + 1]);
    asm volatile("" : "+v"(v));
    SBAR();
#pragma unroll
    for (int d = 0; d < 4; ++d) w[d] = pk_bf16(b[2 * d], b[2 * d + 1]);
    asm volatile("" : "+v"(w));
    SBAR();
}

DI const __bf16* group_src(int cg, int a, const __bf16* wqkv, const __bf16* w1, const __bf16* w3) {
    if (a == 4) return wqkv + 512 + 64 * cg;
    if (a >= 2 && cg >= 12) return wqkv + 128 * (cg - 12) + 64 * (a - 2);
    return ((a & 1) ? w3 : w1) + 64 * cg + 1024 * (a >> 1);
}

DI unsigned kv_flag(int h, int l) { return WS_FQ + (((h >> 2) + (l & 16) / 2) * 16 + (l & 15)) * FS; }
DI unsigned q_flag(int h, int qb, int l) { return WS_FQQ + ((h >> 3) * 16 + 2 * qb + (l & 1)) * FS; }
DI u64 p1_loader(const __bf16* x, const __bf16* wqkv, const __bf16* w1, const __bf16* w3, char* ws, int cg, int rb, int h, int qb, int lw SARG) {
    const int lane = lane_id();
    __builtin_amdgcn_s_setprio(1);
    STAMP(1, 4);
    u32x4 xr[4][5], xe[4][3];
    const char* xb = (const char*)(x + (size_t)(rb * 128 + lw * 32) * DM);
    const unsigned xvo = (lane >> 3) * 1024 + (lane & 7) * 16;
    auto ldx = [&](int i) {
#pragma unroll
        for (int u = 0; u < 5; ++u) asm volatile("global_load_dwordx4 %0, %1, %2 offset:%3" : "=v"(xr[i][u]) : "v"(xvo), "s"(xb + i * 8192), "i"(384 + u * 128) : "memory");
    };
    ldx(0); ldx(1);
    STAMP(1, 5);
    const int ld2 = cg < 12 ? FF : NQKV, r8 = lane >> 3, krow = (r8 & 3) + 8 * (r8 >> 2) + 16 * (lw & 1) + 4 * (lw >> 1);
    const unsigned cho = ((lane & 7) ^ (((r8 >> 1) & 3) << 1)) * 16;
    unsigned vo0 = (krow + 448) * FF * 2 + cho, vo2 = krow * ld2 * 2 + cho, vo4 = krow * NQKV * 2 + cho;
    const __bf16* wb[5];
#pragma unroll
    for (int a = 0; a < 5; ++a) wb[a] = group_src(cg, a, wqkv, w1, w3);
    auto dma = [&](unsigned vo, const void* base, unsigned ldsa) {
        asm volatile("s_mov_b32 m0, %2\n\ts_nop 0\n\tglobal_load_lds_dwordx4 %0, %1" ::"v"(vo), "s"(base), "s"(ldsa) : "memory");
    };
    auto ldw1 = [&](int v) {
#pragma unroll
        for (int kk = 0; kk < 2; ++kk) {
            const unsigned d = (v & 3) * L_WSLOT + kk * 12288 + lw * 1024;
            dma(vo2, wb[2], d); dma(vo2, wb[3], d + 4096); dma(vo4, wb[4], d + 8192);
            vo2 += 64 * ld2; vo4 += 64 * NQKV;
        }
    };
    auto ldw2 = [&](int n) {
        const unsigned vh = vo0 + 64 * FF;
        dma(vo0, wb[0], P2W(n, 0) + lw * 1024); dma(vo0, wb[1], P2W(n, 1) + lw * 1024);
        dma(vh, wb[0], P2W(n, 0) + 4096 + lw * 1024); dma(vh, wb[1], P2W(n, 1) + 4096 + lw * 1024);
        vo0 -= 128 * FF;
    };
    STAMP(1, 6);
#define XWAIT(n, i) asm volatile("s_waitcnt vmcnt(" #n ")" : "+v"(xr[i][0]), "+v"(xr[i][1]), "+v"(xr[i][2]), "+v"(xr[i][3]), "+v"(xr[i][4]))
#define LBAR(n) do { SBAR(); asm volatile("s_waitcnt vmcnt(" #n ") lgkmcnt(0)\n\ts_barrier" ::: "memory"); SBAR(); } while (0)
    const unsigned xw0 = L_X + lw * 4096 + (lane >> 3) * 128 + (((lane & 7) ^ (lane >> 4)) * 16), xw1 = xw0 ^ 64;
    auto putx = [&](int u, int slot) {
#pragma unroll
        for (int i = 0; i < 4; ++i) *(LAS u32x4*)((lptr)((i & 1) ? xw1 : xw0) + slot * L_XSLOT + i * 1024) = u < 3 ? xe[i][u < 3 ? u : 0] : xr[i][u < 3 ? 0 : u - 3];
        SBAR();
    };
    const unsigned sta = L_ST + (lw * 32 + (lane >> 3)) * 16;
    ldx(2);
    XWAIT(10, 0); ldx(3); ln_partial(xr[0], sta + 8); SBAR();
    XWAIT(10, 1); ldw1(0); ln_partial(xr[1], sta + 136); SBAR();
    STAMP(1, 0);
    XWAIT(11, 2); ldw1(1); ln_partial(xr[2], sta + 264); SBAR();
    XWAIT(12, 3); ln_partial(xr[3], sta + 392);
    STAMP(1, 1);
    LBAR(12);
    ldw1(2);
    STAMP(1, 7);
    float nm[4];
#pragma unroll
    for (int i = 0; i < 4; ++i) nm[i] = ln_negmean(sta + i * 128);
    SBAR();
#pragma unroll
    for (int u = 0; u < 5; ++u) { ln_center2(xr[0][u], nm[0], xr[1][u], nm[1]); ln_center2(xr[2][u], nm[2], xr[3][u], nm[3]); }
    __builtin_amdgcn_s_setprio(0);
    LBAR(12);
    STAMP(1, 2);
    ldw1(3);
#pragma unroll
    for (int u = 0; u < 3; ++u)
#pragma unroll
        for (int i = 0; i < 4; ++i) { xe[i][u] = lds_ld16((i & 1) ? xw1 : xw0, u * L_XSLOT + i * 1024); }
    __builtin_amdgcn_s_waitcnt(0xC07F);
    LBAR(12);
    LBAR(6); putx(3, 0); ldw1(4);
    LBAR(6); putx(4, 1); ldw1(5);
    LBAR(6); putx(5, 2); ldw1(6);
    LBAR(6); putx(6, 0); ldw1(7);
    LBAR(6); putx(7, 1); ldw2(0);
    LBAR(4); ldw2(1); ldw2(2);
    LBAR(8); ldw2(3);
    LBAR(8); ldw2(4); ldw2(5);
    LBAR(12); putx(4, 1);
    LBAR(8); putx(3, 0); ldw2(6);
    LBAR(8); putx(2, 2); ldw2(7);
    LBAR(8); putx(1, 1);
    LBAR(4); putx(0, 0);
    u64 fl;
    {
        const int l2 = lane_id();
        asm volatile("global_load_dwordx2 %0, %1, %2 sc1" : "=v"(fl) : "v"(l2 < 32 ? kv_flag(h, l2) : q_flag(h, qb, l2)), "s"(ws) : "memory");
    }
    LBAR(1);
    asm volatile("s_waitcnt vmcnt(0)" : "+v"(fl));
    STAMP(1, 3);
    return __ballot(fl == ld_tag());
}

#define TSTEP(k)
DI void p1_compute(char* ws, int cg, int rb, int w, u32x4 (&xr)[2][3], u32x4 (&h0)[2], u32x4 (&h1)[2] SARG) {
    const int mh = w >> 2, nq = w & 3;
    int lane = lane_id(), fr = lane & 15, g = lane >> 4;
    const int q = (lane >> 2) & 3, p = lane & 3;
    unsigned xa = L_X + (mh * 64 + fr) * 128 + ((g ^ (fr >> 1)) * 16);
    unsigned wa = (8 * (g >> 1) + 4 * (g & 1) + q) * 128 + ((nq ^ (2 * (g & 1) + (q >> 1))) * 32) + 8 * p;
    f32x4 acc[4][5];
#pragma unroll
    for (int i = 0; i < 4; ++i)
#pragma unroll
        for (int t = 0; t < 5; ++t) acc[i][t] = f32x4{0.f, 0.f, 0.f, 0.f};
    u32x4 xf[4], xn[4], wf[3], wn[3];
    auto rdx = [&](u32x4(&d)[4], unsigned a, int off) {
#pragma unroll
        for (int i = 0; i < 4; ++i) d[i] = lds_ld16(a, off + i * 2048);
    };
#pragma unroll
    for (int i = 0; i < 4; ++i)
#pragma unroll
        for (int t = 2; t < 5; ++t) asm volatile("" : "+v"(acc[i][t]));
    asm volatile("" : "+v"(xa), "+v"(wa));
    SBAR();
    {
        const unsigned sta = L_ST + (w * 16 + (lane >> 3)) * 16;
        const unsigned xw = L_X + w * 2048 + (lane >> 3) * 128 + (((lane & 7) ^ (lane >> 4)) * 16);
        asm volatile("s_waitcnt vmcnt(3)" : "+v"(xr[0][0]), "+v"(xr[0][1]), "+v"(xr[0][2]));
        ln_partial(xr[0], sta);
        asm volatile("s_waitcnt vmcnt(0)" : "+v"(xr[1][0]), "+v"(xr[1][1]), "+v"(xr[1][2]));
        ln_partial(xr[1], sta + 128);
        __builtin_amdgcn_s_waitcnt(0xC07F);
        SBAR(); BAR(); SBAR();
        const float nm0 = ln_negmean(sta), nm1 = ln_negmean(sta + 128);
        SBAR();
#pragma unroll
        for (int u = 0; u < 3; ++u) {
            ln_center2(xr[0][u], nm0, xr[1][u], nm1);
            *(LAS u32x4*)((lptr)xw + u * L_XSLOT) = xr[0][u];
            *(LAS u32x4*)((lptr)(xw ^ 64) + u * L_XSLOT + 1024) = xr[1][u];
        }
        if (w < 2) *(LAS float*)(L_SD + (w * 64 + lane) * 4) = ln_std(L_ST + (w * 64 + lane) * 16);
        __builtin_amdgcn_s_waitcnt(0xC07F);
    }
    STAMP(0, 1);
    SBAR(); BAR(); SBAR();
    STAMP(0, 2);
    rdx(xf, xa, 0);
#pragma unroll
    for (int t = 0; t < 3; ++t) wf[t] = lds_tr16(wa, t * 4096, t * 4096 + 2048);
    float sd[4];
#pragma unroll
    for (int i = 0; i < 4; ++i) sd[i] = *(const LAS float*)(L_SD + (mh * 64 + i * 16 + fr) * 4);
    const unsigned wa0 = wa, xa0 = xa;
    auto half1 = [&](u32x4(&xc)[4], u32x4(&wc)[3], u32x4(&xd)[4], u32x4(&wd)[3], int woff) {
        wd[0] = lds_tr16(wa, woff, woff + 2048);
        rdx(xd, xa, 0);
        SBAR();
#pragma unroll
        for (int t = 0; t < 3; ++t) {
#pragma unroll
            for (int i = 0; i < 4; ++i) acc[i][t + 2] = mfma16(wc[t], xc[i], acc[i][t + 2]);
            SBAR();
            if (t < 2) wd[t + 1] = lds_tr16(wa, woff + (t + 1) * 4096, woff + (t + 1) * 4096 + 2048);
            SBAR();
        }
    };
    int xslot = 0;
    for (int v = 0; v < 7; ++v) {
        xa ^= 64;
        asm volatile("" : "+v"(xa));
        SBAR();
        half1(xf, wf, xn, wn, 12288);
        wa += (v & 3) == 3 ? -3 * L_WSLOT : L_WSLOT;
        xa = (xa ^ 64) + (xslot == 2 ? -2 * L_XSLOT : L_XSLOT);
        xslot = xslot == 2 ? 0 : xslot + 1;
        asm volatile("" : "+v"(xa), "+v"(wa));
        SBAR(); BAR(); SBAR();
        TSTEP(v);
        half1(xn, wn, xf, wf, 0);
    }
    xa ^= 64;
    asm volatile("" : "+v"(xa));
    SBAR();
    half1(xf, wf, xn, wn, 12288);
    SBAR(); BAR(); SBAR();
    TSTEP(7);
    wf[0] = lds_tr16(wa0, P2W(0, 0), P2W(0, 0) + 2048); wf[1] = lds_tr16(wa0, P2W(0, 1), P2W(0, 1) + 2048);
    rdx(xf, xa0, L_XSLOT);
    SBAR();
#pragma unroll
    for (int t = 2; t >= 0; --t)
#pragma unroll
        for (int i = 0; i < 4; ++i) acc[i][t + 2] = mfma16(wn[t], xn[i], acc[i][t + 2]);
    SBAR();
    STAMP(0, 3);
    lane = lane_id(); fr = lane & 15; g = lane >> 4;
    const rsrc_t rs = mk_rsrc(ws, WS_END);
    const int tok0 = rb * 128 + mh * 64;
    const unsigned kvb = WS_KV + (tok0 >> 5) * 2048 + (g >> 1) * 512;
    float rs_[4];
#pragma unroll
    for (int i = 0; i < 4; ++i) rs_[i] = __builtin_amdgcn_rcpf(sd[i]);
    if (cg >= 8) {
        const unsigned tw = L_VT + w * 1024 + fr * 32 + g * 8, tr = L_VT + w * 1024 + ((g & 1) * 16 + (g >> 1) * 8 + ((lane >> 2) & 3)) * 32 + (lane & 3) * 8;
#pragma unroll
        for (int i = 0; i < 4; i += 2) {
            *(LAS u32x2*)((lptr)tw) = pack4(acc[i][4], rs_[i]);
            *(LAS u32x2*)((lptr)tw + 512) = pack4(acc[i + 1][4], rs_[i + 1]);
            asm volatile("" ::: "memory");
            const u32x4 o = lds_tr16(tr, 0, 128);
            asm volatile("" ::: "memory");
            st16(o, rs, kvb + (4 * (cg - 8) + nq) * 131072 + i * 1024 + 1024 + (g & 1) * 256 + fr * 16);
        }
    } else {
        const int h = 4 * cg + nq;
        u32x4 o[2];
#pragma unroll
        for (int i = 0; i < 2; ++i) {
            o[i] = widen(pack4(acc[2 * i][4], rs_[2 * i]), pack4(acc[2 * i + 1][4], rs_[2 * i + 1]));
            st16(o[i], rs, kvb + h * 131072 + i * 2048 + (g & 1) * 256 + fr * 16);
        }
        SBAR();
        float km = fmaxf(sum32(dot2(o[0][0], o[0][0], dot2(o[0][1], o[0][1], dot2(o[0][2], o[0][2], dot2(o[0][3], o[0][3], 0.f))))),
                         sum32(dot2(o[1][0], o[1][0], dot2(o[1][1], o[1][1], dot2(o[1][2], o[1][2], dot2(o[1][3], o[1][3], 0.f))))));
        km = fmaxf(km, dpp_f<0xB1>(km)); km = fmaxf(km, dpp_f<0x4E>(km)); km = fmaxf(km, dpp_f<0x141>(km)); km = fmaxf(km, dpp_f<0x140>(km));
        km = max16(km);
        if (lane == 0) __builtin_amdgcn_raw_buffer_store_b32(__float_as_uint(km), rs, WS_KM + (h * 32 + (tok0 >> 6)) * FS, 0, SC1);
    }
    SBAR();
    if (cg >= 12) {
#pragma unroll
        for (int t = 2; t < 4; ++t)
#pragma unroll
            for (int i = 0; i < 4; i += 2)
                st16(widen(pack4(acc[i][t], QSCALE * rs_[i]), pack4(acc[i + 1][t], QSCALE * rs_[i + 1])), rs,
                     WS_Q + (8 * (cg - 12) + 4 * (t - 2) + nq) * 65536 + (tok0 + (i + (g & 1)) * 16 + fr) * 32 + (g >> 1) * 16);
    }
    SBAR();
    STAMP(0, 6);
    const unsigned xb0 = xa0 ^ 64;
    auto glu1 = [&](int t, int i, int r) {
        const float gt = acc[i][t][r], v2 = sd[i] * sd[i];
        acc[i][t][r] = gt * acc[i][t + 1][r] * __builtin_amdgcn_rcpf(fmaf(__builtin_amdgcn_exp2f(-LOG2E * rs_[i] * gt), v2, v2));
    };
    auto half2 = [&](u32x4(&xc)[4], u32x4(&wc)[3], u32x4(&xd)[4], u32x4(&wd)[3], bool rd, int w0o, int w1o, unsigned xadr, int xo, int e) {
        if (rd) {
            wd[0] = lds_tr16(wa0, w0o, w0o + 2048);
            rdx(xd, xadr, xo);
            wd[1] = lds_tr16(wa0, w1o, w1o + 2048);
        }
        SBAR();
#pragma unroll
        for (int t = 0; t < 2; ++t)
#pragma unroll
            for (int i = 0; i < 4; ++i) acc[i][t] = mfma16(wc[t], xc[i], acc[i][t]);
        glu1(2, e >> 2, e & 3);
        asm volatile("" : "+v"(acc[e >> 2][2]));
        SBAR();
    };
#pragma unroll
    for (int n = 0; n < 8; ++n) {
        half2(xf, wf, xn, wn, true, P2W(n, 0) + 4096, P2W(n, 1) + 4096, xb0, ((7 + 2 * n) % 3) * L_XSLOT, 2 * n);
        if (n == 7) { __builtin_amdgcn_s_waitcnt(0xC07F); asm volatile("" ::: "memory"); lds_inc(L_CD); SBAR(); }
        else { BAR(); SBAR(); }
        TSTEP(8 + n);
        half2(xn, wn, xf, wf, n < 7, P2W(n + 1, 0), P2W(n + 1, 1), xa0, ((9 + 2 * n) % 3) * L_XSLOT, 2 * n + 1);
        if (n == KVN) {
            if (cg >= 12) WAIT_VM(4); else WAIT_VM(0);
            arrive(ws, L_CQ, 8, WS_FQ + (cg * 16 + rb) * FS, lane);
            SBAR();
        }
        if (n == QN && cg >= 12) { WAIT_VM(0); arrive(ws, L_CQQ, 8, WS_FQQ + ((cg - 12) * 16 + rb) * FS, lane); SBAR(); }
    }
    STAMP(0, 4);
#pragma unroll
    for (int e = 0; e < 16; ++e) glu1(0, e >> 2, e & 3);
#pragma unroll
    for (int i = 0; i < 2; ++i) {
        h0[i] = widen(pack4(acc[2 * i][0], 1.f), pack4(acc[2 * i + 1][0], 1.f));
        h1[i] = widen(pack4(acc[2 * i][2], 1.f), pack4(acc[2 * i + 1][2], 1.f));
    }
    asm volatile("" : "+v"(h0[0]), "+v"(h0[1]), "+v"(h1[0]), "+v"(h1[1]));
    STAMP(0, 5);
}

#define PRIOSW 3
constexpr float SAFE_SPAN = 80.f;

DI void p2_attn(char* ws, int h, int qb, int w, int cg, int rb, const u32x4 (&h0)[2], const u32x4 (&h1)[2] SARG) {
    const int lane = lane_id();
    const int kh = w >> 2, qp = w & 3, r = lane & 31, hh = lane >> 5;
    lds_wait4(L_KV1);
    STAMP(2, 0);
    {
        const rsrc_t rs = mk_rsrc(ws, WS_END);
        const unsigned o = WS_A2 + ((rb * 8 + kh * 4 + ((lane >> 4) & 1)) * NFT + 32 + 4 * cg + qp) * 512 + (lane & 15) * 32 + hh * 16;
        st16(h0[0], rs, o); st16(h0[1], rs, o + 2 * NFT * 512);
        if (cg < 12) { st16(h1[0], rs, o + 64 * 512); st16(h1[1], rs, o + (2 * NFT + 64) * 512); }
    }
    const u32x4 qa = lds_ld16(L_Q + qp * 2048 + r * 32 + hh * 16, 0), qbf = lds_ld16(L_Q + qp * 2048 + r * 32 + hh * 16, 1024);
    float km = *(const LAS float*)(L_KM + r * 4);
    asm volatile("" ::: "memory");
    if (lane == 0) *(volatile LAS unsigned*)(L_MF + w * 4) = 1u;
    km = fmaxf(km, dpp_f<0xB1>(km)); km = fmaxf(km, dpp_f<0x4E>(km)); km = fmaxf(km, dpp_f<0x141>(km)); km = fmaxf(km, dpp_f<0x140>(km));
    km = max16(km);
    float na = dot2(qa[0], qa[0], dot2(qa[1], qa[1], dot2(qa[2], qa[2], dot2(qa[3], qa[3], 0.f))));
    float nb = dot2(qbf[0], qbf[0], dot2(qbf[1], qbf[1], dot2(qbf[2], qbf[2], dot2(qbf[3], qbf[3], 0.f))));
    na = sum32(na); nb = sum32(nb);
    float ma = __builtin_amdgcn_sqrtf(na * km) * 1.002f + 1e-3f, mb = __builtin_amdgcn_sqrtf(nb * km) * 1.002f + 1e-3f;
    const bool fast = __all(2.f * ma <= SAFE_SPAN && 2.f * mb <= SAFE_SPAN);
    f32x16 oa, ob;
#pragma unroll
    for (int i = 0; i < 16; ++i) { oa[i] = 0.f; ob[i] = 0.f; }
    const unsigned cst = (r == 16 || r == 20) ? 0x3f803f80u : 0u;
    u32x4 v0 = {cst, cst, cst, cst}, v1 = v0;
    unsigned adr = kh * 65536 + lane * 16;
    const u64 vmask = 0x0000FFFF0000FFFFull;
    if (kh) __builtin_amdgcn_s_setprio(1); else __builtin_amdgcn_s_setprio(2);
    if (fast) {
        f32x16 ca, cb;
#pragma unroll
        for (int i = 0; i < 16; ++i) { ca[i] = -ma; cb[i] = -mb; }
#define EX(n) "v_exp_f32 v" #n ", v" #n "\n\t"
#define RDV(r, off) "s_mov_b64 exec, %[vm]\n\tds_read_b128 " r ", %[adr] offset:" off "\n\ts_mov_b64 exec, -1\n\t"
        int cnt = 8, tmp;
        asm volatile(
            "ds_read_b128 v[104:107], %[adr]\n\t"
            "s_waitcnt lgkmcnt(0)\n\t"
            "v_mfma_f32_32x32x16_bf16 v[64:79], v[104:107], v[96:99], v[32:47]\n\t"
            "v_mfma_f32_32x32x16_bf16 v[80:95], v[104:107], v[100:103], v[48:63]\n\t"
            RDV("v[108:111]", "1024") RDV("v[112:115]", "1280")
            "ds_read_b128 v[104:107], %[adr] offset:2048\n\t"
            "s_nop 7\n\t"
            EX(64) EX(65) EX(66) EX(67)
            "1:\n\t"
            ".set kvo, 2048\n\t"
            ".rept 4\n\t"
            EX(68) EX(69) EX(70) EX(71) EX(72) EX(73)
            "s_waitcnt lgkmcnt(2)\n\t"
            "v_smfmac_f32_32x32x32_bf16 v[0:15], v[108:111], v[64:71], %[idx]\n\t"
            EX(74) EX(75) EX(76) EX(77) EX(78) EX(79) EX(80) EX(81)
            "s_waitcnt lgkmcnt(1)\n\t"
            "v_smfmac_f32_32x32x32_bf16 v[0:15], v[112:115], v[72:79], %[idx]\n\t"
            EX(82) EX(83)
            "s_waitcnt lgkmcnt(0)\n\t"
            "v_mfma_f32_32x32x16_bf16 v[64:79], v[104:107], v[96:99], v[32:47]\n\t"
            EX(84) EX(85) EX(86) EX(87) EX(88) EX(89)
            "v_smfmac_f32_32x32x32_bf16 v[16:31], v[108:111], v[80:87], %[idx]\n\t"
            RDV("v[108:111]", "kvo+1024")
            EX(90) EX(91) EX(92) EX(93) EX(94) EX(95) EX(64) EX(65)
            "v_smfmac_f32_32x32x32_bf16 v[16:31], v[112:115], v[88:95], %[idx]\n\t"
            RDV("v[112:115]", "kvo+1280")
            EX(66) EX(67)
            "v_mfma_f32_32x32x16_bf16 v[80:95], v[104:107], v[100:103], v[48:63]\n\t"
            "ds_read_b128 v[104:107], %[adr] offset:kvo+2048\n\t"
            ".set kvo, kvo+2048\n\t"
            ".endr\n\t"
            "v_add_u32 %[adr], 8192, %[adr]\n\t"
            "s_cmp_lg_u32 %[cnt], 8\n\t"
            "s_cbranch_scc1 2f\n\t"
            "s_waitcnt vmcnt(0)\n\t"
            "s_mov_b64 exec, 1\n\t"
            "ds_add_u32 %[fl], %[one] offset:%[cho]\n\t"
            "s_mov_b64 exec, -1\n\t"
            "s_mov_b32 m0, 4000000\n\t"
            "3:\n\t"
            "ds_read_b128 v[116:119], %[fl]\n\t"
            "s_waitcnt lgkmcnt(0)\n\t"
            "v_and_b32 v116, v116, v117\n\t"
            "v_and_b32 v118, v118, v119\n\t"
            "v_and_b32 v116, v116, v118\n\t"
            "s_nop 1\n\t"
            "v_readfirstlane_b32 %[tmp], v116\n\t"
            "s_sub_u32 m0, m0, 1\n\t"
            "s_cbranch_scc1 2f\n\t"
            "s_cmp_eq_u32 %[tmp], 0\n\t"
            "s_cbranch_scc1 3b\n\t"
            "2:\n\t"
            "s_cmp_lg_u32 %[cnt], %[sw]\n\t"
            "s_cbranch_scc1 4f\n\t"
            "s_setprio 0\n\t"
            "4:\n\t"
            "s_sub_u32 %[cnt], %[cnt], 1\n\t"
            "s_cmp_lg_u32 %[cnt], 0\n\t"
            "s_cbranch_scc1 1b\n\t"
            "s_waitcnt lgkmcnt(0)\n\t"
            "s_nop 15\n\t"
            : "+{v[0:15]}"(oa), "+{v[16:31]}"(ob), "+{v[108:111]}"(v0), "+{v[112:115]}"(v1), [adr] "+v"(adr), [cnt] "+s"(cnt), [tmp] "=&s"(tmp)
            : "{v[32:47]}"(ca), "{v[48:63]}"(cb), "{v[96:99]}"(qa), "{v[100:103]}"(qbf), [idx] "v"(0xDDDDDDDDu), [vm] "s"(vmask), [fl] "v"(L_KV2), [one] "v"(1), [cho] "i"(L_CH - L_KV2)
              , [sw] "s"(kh ? -1 : PRIOSW)

            : "v64", "v65", "v66", "v67", "v68", "v69", "v70", "v71", "v72", "v73", "v74", "v75", "v76", "v77", "v78", "v79", "v80", "v81", "v82", "v83",
              "v84", "v85", "v86", "v87", "v88", "v89", "v90", "v91", "v92", "v93", "v94", "v95", "v104", "v105", "v106", "v107", "v116", "v117", "v118",
              "v119", "scc", "memory");
    } else {
        WAIT_VM(0);
        lds_inc(L_CH);
        lds_wait4(L_KV2);
        ma = -INFINITY; mb = -INFINITY;
        for (int t = 0; t < 32; ++t) {
            u32x4 k0;
            asm volatile("ds_read_b128 %0, %3\n\ts_mov_b64 exec, %4\n\tds_read_b128 %1, %3 offset:1024\n\tds_read_b128 %2, %3 offset:1280\n\t"
                         "s_mov_b64 exec, -1\n\ts_waitcnt lgkmcnt(0)" : "=&v"(k0), "+v"(v0), "+v"(v1) : "v"(adr), "s"(vmask));
            f32x16 z;
#pragma unroll
            for (int i = 0; i < 16; ++i) z[i] = 0.f;
            f32x16 sa = mfma32(k0, qa, z), sb = mfma32(k0, qbf, z);
            float xa = sa[0], xb = sb[0];
#pragma unroll
            for (int i = 1; i < 16; ++i) { xa = fmaxf(xa, sa[i]); xb = fmaxf(xb, sb[i]); }
            xa = fmaxf(max32(xa), ma); xb = fmaxf(max32(xb), mb);
            const float fa = __builtin_amdgcn_exp2f(ma - xa), fb = __builtin_amdgcn_exp2f(mb - xb);
            ma = xa; mb = xb;
#pragma unroll
            for (int i = 0; i < 16; ++i) { sa[i] = __builtin_amdgcn_exp2f(sa[i] - xa); sb[i] = __builtin_amdgcn_exp2f(sb[i] - xb); oa[i] *= fa; ob[i] *= fb; }
            oa = pv(v0, lo8(sa), oa); oa = pv(v1, hi8(sa), oa); ob = pv(v0, lo8(sb), ob); ob = pv(v1, hi8(sb), ob);
            adr += 2048;
        }
    }
    STAMP(2, 1);
    lds_inc(L_AD);
    {
        lds_wait4(L_MF); lds_wait4(L_MF + 16);
        LAS float* sx = (LAS float*)(L_MRG + w * 2560) + lane;
#pragma unroll
        for (int i = 0; i < 9; ++i) sx[i * 64] = kh ? oa[i] : ob[i];
        sx[9 * 64] = kh ? ma : mb;
        asm volatile("s_waitcnt lgkmcnt(0)" ::: "memory");
        if (lane == 0) *(volatile LAS unsigned*)(L_MF + 32 + w * 4) = 1u;
        for (int spin = 0; spin < 4000000; ++spin) {
            if (rfl(*(volatile LAS unsigned*)(L_MF + 32 + (w ^ 4) * 4))) break;
            __builtin_amdgcn_s_sleep(1);
        }
        asm volatile("" ::: "memory");
    }
    {
        const LAS float* sx = (const LAS float*)(L_MRG + (w ^ 4) * 2560) + lane;
        const float m1 = kh ? mb : ma, m2 = sx[9 * 64], mm = fmaxf(m1, m2);
        const float f1 = __builtin_amdgcn_exp2f(m1 - mm), f2 = __builtin_amdgcn_exp2f(m2 - mm);
        float o[9];
#pragma unroll
        for (int i = 0; i < 9; ++i) o[i] = (kh ? ob[i] : oa[i]) * f1 + sx[i * 64] * f2;
        const float inv = __builtin_amdgcn_rcpf(o[8]);
        unsigned a0 = pk_bf16(o[0] * inv, o[1] * inv), a1 = pk_bf16(o[2] * inv, o[3] * inv);
        unsigned b0 = pk_bf16(o[4] * inv, o[5] * inv), b1 = pk_bf16(o[6] * inv, o[7] * inv);
        auto s0 = __builtin_amdgcn_permlane32_swap(a0, b0, false, false);
        auto s1 = __builtin_amdgcn_permlane32_swap(a1, b1, false, false);
        const int tok = qb * 256 + qp * 64 + kh * 32 + r;
        st16(u32x4{s0[0], s1[0], s0[1], s1[1]}, mk_rsrc(ws, WS_END), WS_A2 + ((tok >> 4) * NFT + h) * 512 + (tok & 15) * 32 + hh * 16);
    }
    WAIT_VM(0);
    arrive(ws, L_CO, 8, WS_FO + (qb * 32 + h) * FS, lane);
    STAMP(2, 2);
}

DI void p2_gemm(const __bf16* x, const __bf16* wo, const __bf16* w2, char* ws, __bf16* out, int h, int qb, int cg, int rb, int lw, u64 seen SARG) {
    const int lane = lane_id();
    const u64 tag = ld_tag();
    auto dma = [&](const char* base, unsigned vo, unsigned ldsa) {
        asm volatile("s_mov_b32 m0, %2\n\ts_nop 0\n\tglobal_load_lds_dwordx4 %0, %1 sc1" ::"v"(vo), "s"(base), "s"(ldsa) : "memory");
    };
    const int xcd = blockIdx.x & 7, j = blockIdx.x >> 3, rb4 = xcd * 4 + (j >> 3), cb = j & 7;
    const rsrc_t rs = mk_rsrc(ws, WS_END);
    {
        const char* kvg = ws + WS_KV + h * 131072;
        const char* qg = ws + WS_Q + h * 65536 + qb * 8192 + lw * 2048;
        unsigned vo = (lw + 8 * qb) * 1024 + lane * 16;
        auto kv4 = [&](int n) {
            dma(kvg, vo, (4 * n + lw) * 1024); dma(kvg + 65536, vo, (64 + 4 * n + lw) * 1024);
            vo = (vo + 4096) & 65535;
        };
        if ((unsigned)seen != 0xffffffffu) poll_flags(ws, kv_flag(h, lane), 32, tag, lane);
        if (lw == 2) asm volatile("s_mov_b32 m0, %2\n\ts_nop 0\n\tglobal_load_lds_dword %0, %1 sc1" ::"v"((lane & 31) * FS), "s"(ws + WS_KM + h * 32 * FS), "s"(L_KM) : "memory");
        if ((seen >> 32) != 0xffffffffu) {
            kv4(0); kv4(1); kv4(2); kv4(3);
            poll_flags(ws, q_flag(h, qb, lane), 2, tag, lane);
            dma(qg, lane * 16, L_Q + lw * 2048); dma(qg + 1024, lane * 16, L_Q + lw * 2048 + 1024);
        } else {
            dma(qg, lane * 16, L_Q + lw * 2048); dma(qg + 1024, lane * 16, L_Q + lw * 2048 + 1024);
            kv4(0); kv4(1); kv4(2); kv4(3);
        }
        STAMP(3, 0);
        __builtin_amdgcn_s_setprio(3);
        WAIT_VM(0);
        if (lane == 0) *(volatile LAS unsigned*)(L_KV1 + lw * 4) = 1u;
        lds_wait_eq(L_CD, 8);
#pragma unroll
        for (int n = 4; n < 16; ++n) kv4(n);
    }
    WAIT_VM(0);
    if (lane == 0) *(volatile LAS unsigned*)(L_KV2 + lw * 4) = 1u;
    STAMP(3, 7);
    {
        unsigned tv[3][8];
#pragma unroll
        for (int f = 0; f < 3; ++f) {
            const int idx = j * 9 + min(lw + 4 * f, 8), ft = idx >> 1, n = xcd * 64 + (idx & 1) * 32 + (lane & 31);
            const unsigned short* src = (const unsigned short*)(ft < 32 ? wo + ft * 16 * DM : w2 + (ft - 32) * 16 * DM) + (lane >> 5) * 8 * DM + n;
#pragma unroll
            for (int d = 0; d < 8; ++d) tv[f][d] = src[d * DM];
        }
#pragma unroll
        for (int f = 0; f < 3; ++f) asm volatile("" : "+v"(tv[f][0]), "+v"(tv[f][1]), "+v"(tv[f][2]), "+v"(tv[f][3]), "+v"(tv[f][4]), "+v"(tv[f][5]), "+v"(tv[f][6]), "+v"(tv[f][7]));
#pragma unroll
        for (int f = 0; f < 3; ++f)
            if (lw + 4 * f < 9)
                st16(u32x4{tv[f][0] | (tv[f][1] << 16), tv[f][2] | (tv[f][3] << 16), tv[f][4] | (tv[f][5] << 16), tv[f][6] | (tv[f][7] << 16)}, rs,
                     WS_WT + (xcd * 288 + j * 9 + lw + 4 * f) * 1024 + lane * 16);
    }
    WAIT_VM(0);
    arrive(ws, L_CW, 4, WS_FW + (xcd * 32 + j) * FS, lane);
    if (lw == 3) { lds_wait_eq(L_CH, 8); if (lane == 0) set_flag(ws, WS_FH + (rb * 16 + cg) * FS, tag); }
    STAMP(3, 1);
    const int r = lane & 31, hh = lane >> 5;
    const unsigned va = WS_A2 + (rb4 * 4 + (r >> 4)) * NFT * 512 + (r & 15) * 32 + hh * 16, vw = WS_WT + cb * 288 * 1024 + lane * 16;
    f32x16 acc[2][2];
#pragma unroll
    for (int i = 0; i < 16; ++i) { acc[0][0][i] = 0.f; acc[0][1][i] = 0.f; acc[1][0][i] = 0.f; acc[1][1][i] = 0.f; }
    u32x4 a0[4], w0[4], a1[4], w1[4];
    auto ld = [&](u32x4(&a)[4], u32x4(&w)[4], int ks) {
#pragma unroll
        for (int e = 0; e < 4; ++e) {
            w[e] = __builtin_amdgcn_raw_buffer_load_b128(rs, vw + e * 1024, ks * 4096, SC1);
            a[e] = __builtin_amdgcn_raw_buffer_load_b128(rs, va + (e & 1) * 2 * NFT * 512 + (e >> 1) * 512, ks * 1024, SC1);
        }
    };
#define PACE 1
    auto mm = [&](u32x4(&a)[4], u32x4(&w)[4], bool pace) {
#pragma unroll
        for (int e = 0; e < 8; ++e) {
            acc[e & 1][(e >> 1) & 1] = mfma32(w[(e >> 2) * 2 + ((e >> 1) & 1)], a[(e >> 2) * 2 + (e & 1)], acc[e & 1][(e >> 1) & 1]);
            if (pace) { SBAR(); __builtin_amdgcn_s_sleep(PACE); SBAR(); }
        }
    };
    wait_flags(ws, lane < 32 ? WS_FW + (cb * 32 + lane) * FS : WS_FH + ((rb4 >> 1) * 16 + (lane & 15)) * FS, 48, tag, lane, lw, L_G + 4);
    STAMP(3, 2);
#define DEFER 6
    ld(a0, w0, 16 + lw); ld(a1, w1, 20 + lw);
#pragma unroll 1
    for (int it = 0; it < DEFER; ++it) {
        mm(a0, w0, true); SBAR(); ld(a0, w0, 24 + lw + 8 * it); SBAR();
        mm(a1, w1, true); SBAR(); ld(a1, w1, 28 + lw + 8 * it); SBAR();
    }
    if (DEFER < 6) lds_wait_ge(L_AD, 4);
#pragma unroll 1
    for (int it = DEFER; it < 6; ++it) {
        mm(a0, w0, false); SBAR(); ld(a0, w0, 24 + lw + 8 * it); SBAR();
        mm(a1, w1, false); SBAR(); ld(a1, w1, 28 + lw + 8 * it); SBAR();
    }
    mm(a0, w0, false); mm(a1, w1, false);
    STAMP(3, 3);
    const size_t o = (size_t)(rb4 * 64 + (lw >> 1) * 32 + r) * DM + cb * 64 + (lw & 1) * 32 + 4 * hh;
    u32x2 xv[4];
#pragma unroll
    for (int q = 0; q < 4; ++q) xv[q] = *(const u32x2*)(x + o + q * 8);
    {
        const GAS u64* fp = (const GAS u64*)(ws + WS_FO + ((rb4 >> 2) * 32 + (lane & 31)) * FS);
        bool allo = false;
        lds_wait_ge(L_AD, 1);
#pragma unroll 1
        for (int n = 0; n < 4; n += 2) {
            const unsigned need = (0x303u << (8 * n + 2 * lw));
            for (int spin = 0; spin < 400000; ++spin) {
                unsigned ok;
                if (lw == 0 && !allo) {
                    ok = (unsigned)__ballot(__hip_atomic_load(fp, __ATOMIC_RELAXED, __HIP_MEMORY_SCOPE_AGENT) == tag);
                    *(volatile LAS unsigned*)(L_G + 8) = ok;
                    allo = ok == 0xffffffffu;
                } else ok = rfl(*(volatile LAS unsigned*)(L_G + 8));
                if ((ok & need) == need) break;
                __builtin_amdgcn_s_sleep(2);
            }
            asm volatile("" ::: "memory");
            SBAR();
            ld(a0, w0, lw + 4 * n); ld(a1, w1, lw + 4 * n + 4);
            SBAR();
            mm(a0, w0, false); mm(a1, w1, false);
        }
        for (int spin = 0; lw == 0 && !allo && spin < 400000; ++spin) {
            const unsigned ok = (unsigned)__ballot(__hip_atomic_load(fp, __ATOMIC_RELAXED, __HIP_MEMORY_SCOPE_AGENT) == tag);
            *(volatile LAS unsigned*)(L_G + 8) = ok;
            allo = ok == 0xffffffffu;
            __builtin_amdgcn_s_sleep(2);
        }
    }
    STAMP(3, 5);
    BAR();
#pragma unroll
    for (int e = 0; e < 4; ++e)
#pragma unroll
        for (int q = 0; q < 4; ++q)
            *(LAS f32x4*)(lw * 16384 + (e * 4 + q) * 1024 + lane * 16) = f32x4{acc[e >> 1][e & 1][4 * q], acc[e >> 1][e & 1][4 * q + 1], acc[e >> 1][e & 1][4 * q + 2], acc[e >> 1][e & 1][4 * q + 3]};
    __builtin_amdgcn_s_waitcnt(0xC07F);
    BAR();
#pragma unroll
    for (int q = 0; q < 4; ++q) {
        f32x4 s = *(const LAS f32x4*)((lw * 4 + q) * 1024 + lane * 16);
#pragma unroll
        for (int ww = 1; ww < 4; ++ww) s += *(const LAS f32x4*)(ww * 16384 + (lw * 4 + q) * 1024 + lane * 16);
        *(u32x2*)(out + o + q * 8) = u32x2{pk_bf16(s[0] + bf_lo(xv[q][0]), s[1] + bf_hi(xv[q][0])), pk_bf16(s[2] + bf_lo(xv[q][1]), s[3] + bf_hi(xv[q][1]))};
    }
    if (blockIdx.x == 0 && lw == 0 && lane == 0) set_flag(ws, WS_EPOCH, tag);
    STAMP(3, 6);
}

__global__ __launch_bounds__(768, 3) void fused_block(const __bf16* __restrict__ x, const __bf16* __restrict__ wqkv, const __bf16* __restrict__ wo,
                                                      const __bf16* __restrict__ w1, const __bf16* __restrict__ w3, const __bf16* __restrict__ w2,
                                                      char* __restrict__ ws, __bf16* __restrict__ out) {
    SDECL;
    const int w = rfl(threadIdx.x >> 6);
    const int xcd = blockIdx.x & 7, j = blockIdx.x >> 3;
    const int cg = (xcd >> 1) * 4 + (j >> 3), rb = (xcd & 1) * 8 + (j & 7);
    const int h = xcd * 4 + (j >> 3), qb = j & 7;
    if (__builtin_expect(w >= 8, 1)) {
        const u64 seen = p1_loader(x, wqkv, w1, w3, ws, cg, rb, h, qb, w - 8 SPASS);
        p2_gemm(x, wo, w2, ws, out, h, qb, cg, rb, w - 8, seen SPASS);
        SDUMP(1);
        return;
    }
    STAMP(0, 0);
    u32x4 xr[2][3];
    {
        const int lane = lane_id();
        const char* xb = (const char*)(x + (size_t)(rb * 128 + w * 16) * DM);
        const unsigned vo = (lane >> 3) * 1024 + (lane & 7) * 16;
#pragma unroll
        for (int i = 0; i < 2; ++i)
#pragma unroll
            for (int u = 0; u < 3; ++u)
                asm volatile("global_load_dwordx4 %0, %1, %2 offset:%3" : "=v"(xr[i][u]) : "v"(vo), "s"(xb + i * 8192), "i"(u * 128) : "memory");
        if (w == 7) {
            u64 e;
            asm volatile("s_load_dwordx2 %0, %1, 0x0\n\ts_waitcnt lgkmcnt(0)" : "=s"(e) : "s"(ws + WS_EPOCH) : "memory");
            *(LAS unsigned*)(L_CTL + 16 + lane * 4) = 0u;
            *(LAS u64*)L_TAG = e + 1;
        }
    }
    u32x4 h0[2], h1[2];
    p1_compute(ws, cg, rb, w, xr, h0, h1 SPASS);
    p2_attn(ws, h, qb, w, cg, rb, h0, h1 SPASS);
    SDUMP(0);
}

extern "C" void kernel_launch(void* const* d_in, const int* in_sizes, int n_in, void* d_out, int out_size, void* d_ws, size_t ws_size,
                              hipStream_t stream) {
    (void)hipFuncSetAttribute((const void*)fused_block, hipFuncAttributeMaxDynamicSharedMemorySize, LDS_BYTES);
    fused_block<<<256, 768, LDS_BYTES, stream>>>((const __bf16*)d_in[0], (const __bf16*)d_in[1], (const __bf16*)d_in[2], (const __bf16*)d_in[4],
                                                 (const __bf16*)d_in[5], (const __bf16*)d_in[6], (char*)d_ws, (__bf16*)d_out);
}
```
